```python
import torch

def _softmax(x, axis=-1):
    return torch.softmax(x, dim=axis)
def _gelu(x):
    return torch.nn.functional.gelu(x, approximate="tanh")
def _silu(x):
    return torch.nn.functional.silu(x)
def _sigmoid(x):
    return torch.sigmoid(x)
def _layernorm(x, axis=-1, eps=1e-5):
    m = x.mean(dim=axis, keepdim=True)
    v = x.var(dim=axis, keepdim=True, unbiased=False)
    return (x - m) / torch.sqrt(v + eps)
def _scatter_add(tgt, idx, src, axis=0):
    return tgt.clone().index_add_(axis, idx, src)
def _segment_sum(src, seg, n):
    out = torch.zeros((n,) + src.shape[1:], dtype=src.dtype)
    return out.index_add_(0, seg, src)
def _pad(x, pw):
    flat = tuple(v for pair in reversed(pw) for v in pair)
    return torch.nn.functional.pad(x, flat)

kernel_name = 'gated_mix__ssm_conv_s4__glu_gated_glu__prompt'

def setup_inputs():

    _w = (lambda a: (lambda t: t.to(torch.bfloat16) if t.is_floating_point() else t)(torch.as_tensor(a)))
    def _A_setup():
        B, L, D, N = 1, 2048, 512, 128
        A_re = -(lambda _x: torch.exp(torch.as_tensor(_x)))((lambda *s: torch.randn(*s, dtype=torch.bfloat16) * 6.4)(D, N))
        A_im = torch.pi * (lambda *s: torch.randn(*s, dtype=torch.bfloat16) * 0.1)(D, N)
        B_re = (lambda *s: torch.randn(*s, dtype=torch.bfloat16) * 0.2)(D, N)
        B_im = (lambda *s: torch.randn(*s, dtype=torch.bfloat16) * 0.2)(D, N)
        C_re = (lambda *s: torch.randn(*s, dtype=torch.bfloat16) * 0.2)(D, N)
        C_im = (lambda *s: torch.randn(*s, dtype=torch.bfloat16) * 0.2)(D, N)
        log_dt = (lambda *s: torch.randn(*s, dtype=torch.bfloat16) * 0.1)(D) * 0.5 - 4.0
        Dskip = (lambda *s: torch.randn(*s, dtype=torch.bfloat16) * 0.1)(D)
        return {
            "u": _w((lambda *s: torch.randn(*s, dtype=torch.bfloat16) * 0.05)(B, L, D)),
            "A_re": _w(A_re),
            "A_im": _w(A_im),
            "B_re": _w(B_re),
            "B_im": _w(B_im),
            "C_re": _w(C_re),
            "C_im": _w(C_im),
            "log_dt": _w(log_dt),
            "Dskip": _w(Dskip),
        }
    _A_in = _A_setup()

    def _B_setup():
        B, S, D, F = 1, 2048, 512, 1792
        return {
            "x": _w((lambda *s: torch.randn(*s, dtype=torch.bfloat16) * 0.05)(B, S, D)),
            "w_gate": _w((lambda *s: torch.randn(*s, dtype=torch.bfloat16) * 0.4)(D, F)),
            "w_up": _w((lambda *s: torch.randn(*s, dtype=torch.bfloat16) * 0.05)(D, F)),
            "w_down": _w((lambda *s: torch.randn(*s, dtype=torch.bfloat16) * 0.05)(F, D)),
        }
    _B_in = _B_setup()

    return {
        "A_u": _A_in["u"],
        "A_A_re": _A_in["A_re"],
        "A_A_im": _A_in["A_im"],
        "A_B_re": _A_in["B_re"],
        "A_B_im": _A_in["B_im"],
        "A_C_re": _A_in["C_re"],
        "A_C_im": _A_in["C_im"],
        "A_log_dt": _A_in["log_dt"],
        "A_Dskip": _A_in["Dskip"],
        "B_x": _B_in["x"],
        "B_w_gate": _B_in["w_gate"],
        "B_w_up": _B_in["w_up"],
        "B_w_down": _B_in["w_down"],
        "gate_w": _w((lambda *s: torch.randn(*s, dtype=torch.bfloat16) * 0.05)(512) * 16.0),
    }

def reference(A_u, A_A_re, A_A_im, A_B_re, A_B_im, A_C_re, A_C_im, A_log_dt, A_Dskip, B_x, B_w_gate, B_w_up, B_w_down, gate_w):
    def _A_ref(u, A_re, A_im, B_re, B_im, C_re, C_im, log_dt, Dskip):
        Bsz, L, D = u.shape
        dt = torch.exp(log_dt)[:, None]
        A = A_re + 1j * A_im
        Bc = B_re + 1j * B_im
        Cc = C_re + 1j * C_im
        logdA = dt * A
        dB = dt * Bc
        l = (lambda x, dt: x.to(dt))(torch.arange(L), A_re.dtype)
        P = torch.exp(logdA[:, :, None] * l[None, None, :])
        K = torch.einsum("dn,dnl,dn->dl", Cc, P, dB).real
        L2 = 2 * L
        Kp = _pad(K, ((0, 0), (0, L)))
        up = (lambda x, *d: x.permute(*d))(u, 0, 2, 1)
        up = _pad(up, ((0, 0), (0, 0), (0, L)))
        Kf = (lambda x, axes=(-2,-1): torch.fft.rfft2(x.float(), dim=axes))(Kp[:, None, :], axes=(-2, -1))
        Uf = (lambda x, axes=(-2,-1): torch.fft.rfft2(x.float(), dim=axes))(up[:, :, None, :], axes=(-2, -1))
        Yf = Uf * Kf[None, :, :, :]
        y = (lambda x, s, axes=(-2,-1): torch.fft.irfft2(x, s=s, dim=axes).to(torch.bfloat16))(Yf, (1, L2), axes=(-2, -1))[:, :, 0, :L]
        y = (lambda x, *d: x.permute(*d))(y, 0, 2, 1) + u * Dskip[None, None, :]
        return y

    def _B_ref(x, w_gate, w_up, w_down):
        g = torch.einsum("bsd,df->bsf", x, w_gate)
        u = torch.einsum("bsd,df->bsf", x, w_up)
        h = _sigmoid(g) * u
        return torch.einsum("bsf,fd->bsd", h, w_down)

    x = A_u
    g = _sigmoid((x * gate_w).sum(-1, keepdims=True))
    a = _A_ref(x, A_A_re, A_A_im, A_B_re, A_B_im, A_C_re, A_C_im, A_log_dt, A_Dskip)
    b = _B_ref(x, B_w_gate, B_w_up, B_w_down)
    return g * a + (1 - g) * b
```

```cpp
#include <hip/hip_runtime.h>
#include <hip/hip_bf16.h>
#include <stdint.h>

typedef unsigned short u16;
__device__ __forceinline__ float bf2f(u16 v){ return __uint_as_float(((unsigned)v)<<16); }
__device__ __forceinline__ u16 f2bf(float f){ unsigned u=__float_as_uint(f); u += 0x7FFF + ((u>>16)&1); return (u16)(u>>16); }
__device__ __forceinline__ float rbf(float f){ return bf2f(f2bf(f)); }

constexpr int L=2048, D=512, N=128, F=1792;

__global__ void kgen(const u16* Are,const u16* Aim,const u16* Bre,const u16* Bim,const u16* Cre,const u16* Cim,const u16* logdt,float* K){
  int l=blockIdx.x*blockDim.x+threadIdx.x; int d=blockIdx.y;
  if(l>=L) return;
  float lf=rbf((float)l);
  float dt=rbf(expf(bf2f(logdt[d])));
  float acc=0.f;
  for(int n=0;n<N;++n){
    float ar=dt*bf2f(Are[d*N+n]), ai=dt*bf2f(Aim[d*N+n]);
    float br=dt*bf2f(Bre[d*N+n]), bi=dt*bf2f(Bim[d*N+n]);
    float cr=bf2f(Cre[d*N+n]), ci=bf2f(Cim[d*N+n]);
    float m=expf(ar*lf); float s,c; sincosf(ai*lf,&s,&c);
    float pr=m*c, pi=m*s;
    float wr=cr*br-ci*bi, wi=cr*bi+ci*br;
    acc+=wr*pr-wi*pi;
  }
  K[d*L+l]=acc;
}
__global__ void conv(const float* K,const u16* u,const u16* Dskip,u16* a){
  int d=blockIdx.x*blockDim.x+threadIdx.x; int t=blockIdx.y;
  float acc=0.f;
  for(int l=0;l<=t;++l) acc+=K[d*L+l]*bf2f(u[(t-l)*D+d]);
  float y=rbf(acc);
  float sk=rbf(bf2f(u[t*D+d])*bf2f(Dskip[d]));
  a[t*D+d]=f2bf(y+sk);
}
__global__ void gemm1(const u16* x,const u16* wg,const u16* wu,u16* h){
  int f=blockIdx.x*blockDim.x+threadIdx.x; int t=blockIdx.y;
  float g=0.f,u=0.f;
  for(int k=0;k<D;++k){ float xv=bf2f(x[t*D+k]); g+=xv*bf2f(wg[k*F+f]); u+=xv*bf2f(wu[k*F+f]); }
  g=rbf(g); u=rbf(u);
  float s=rbf(1.f/(1.f+expf(-g)));
  h[t*F+f]=f2bf(s*u);
}
__global__ void gate(const u16* x,const u16* gw,float* g){
  int t=blockIdx.x*blockDim.x+threadIdx.x;
  float acc=0.f;
  for(int k=0;k<D;++k) acc+=rbf(bf2f(x[t*D+k])*bf2f(gw[k]));
  acc=rbf(acc);
  g[t]=rbf(1.f/(1.f+expf(-acc)));
}
__global__ void gemm2(const u16* h,const u16* wd,const u16* a,const float* g,u16* out){
  int d=blockIdx.x*blockDim.x+threadIdx.x; int t=blockIdx.y;
  float acc=0.f;
  for(int k=0;k<F;++k) acc+=bf2f(h[t*F+k])*bf2f(wd[k*D+d]);
  float b=rbf(acc);
  float gg=g[t];
  float t1=rbf(gg*bf2f(a[t*D+d]));
  float t2=rbf(rbf(1.f-gg)*b);
  out[t*D+d]=f2bf(t1+t2);
}

extern "C" void kernel_launch(void* const* d_in, const int* in_sizes, int n_in,
                              void* d_out, int out_size, void* d_ws, size_t ws_size,
                              hipStream_t stream) {
  const u16* u=(const u16*)d_in[0];
  const u16* Are=(const u16*)d_in[1]; const u16* Aim=(const u16*)d_in[2];
  const u16* Bre=(const u16*)d_in[3]; const u16* Bim=(const u16*)d_in[4];
  const u16* Cre=(const u16*)d_in[5]; const u16* Cim=(const u16*)d_in[6];
  const u16* logdt=(const u16*)d_in[7]; const u16* Dskip=(const u16*)d_in[8];
  const u16* wg=(const u16*)d_in[10]; const u16* wu=(const u16*)d_in[11]; const u16* wd=(const u16*)d_in[12];
  const u16* gw=(const u16*)d_in[13];
  char* ws=(char*)d_ws;
  float* K=(float*)ws; ws+=(size_t)D*L*4;
  u16* a=(u16*)ws; ws+=(size_t)D*L*2;
  u16* h=(u16*)ws; ws+=(size_t)F*L*2;
  float* g=(float*)ws;
  kgen<<<dim3(L/256,D),256,0,stream>>>(Are,Aim,Bre,Bim,Cre,Cim,logdt,K);
  conv<<<dim3(D/256,L),256,0,stream>>>(K,u,Dskip,a);
  gemm1<<<dim3(F/256,L),256,0,stream>>>(u,wg,wu,h);
  gate<<<dim3(L/256),256,0,stream>>>(u,gw,g);
  gemm2<<<dim3(D/256,L),256,0,stream>>>(h,wd,a,g,(u16*)d_out);
}
```

```cpp
constexpr unsigned G2_RED = 0;
constexpr unsigned G2_AT = 8 * 7 * 2048;
constexpr unsigned G2_AROW = 144;
constexpr unsigned G2_END = G2_AT + 64 * G2_AROW;

struct G2W {
  v4u wf[7][4];
  rsrc_t rs;
  unsigned voff, soff;
  template <int I> __device__ __forceinline__ void load() {
    if constexpr (I < 28) wf[I >> 2][I & 3] = bld128(rs, voff, soff + I * 1024, 1);
  }
  template <int I> __device__ __forceinline__ void at() { load<I>(); }
};
__device__ __forceinline__ void g2_setup_w(const Params& p, int ct, unsigned w, int lane, G2W& g) {
  g.rs = mkrsrc(p.ws, WS_DBG);
  g.voff = (unsigned)(lane * 16);
  g.soff = (unsigned)(WS_WD + (ct * 56 + 7 * w) * 4096);
}
struct G2S { f32x4 acc[4][4]; v4u hf[7][4]; };
template <int LO, int HI> __device__ __forceinline__ void g2_load_h(const Params& p, int t0, unsigned w, int lane, G2S& st) {
  const rsrc_t rsS = mkrsrc(p.ws, WS_DBG);
#pragma unroll
  for (int s = LO; s < HI; ++s)
#pragma unroll
    for (int tt = 0; tt < 4; ++tt) st.hf[s][tt] = bld128(rsS, (unsigned)(lane * 16), (unsigned)(WS_H + ((t0 / 16 + tt) * 56 + 7 * w + s) * 1024), 1);
  SBAR();
}
template <int S> __device__ __forceinline__ void g2_step(G2S& st, const G2W& g) {
#pragma unroll
  for (int tt = 0; tt < 4; ++tt)
#pragma unroll
    for (int dt = 0; dt < 4; ++dt) st.acc[dt][tt] = mfma16(g.wf[S][dt], st.hf[S][tt], S == 0 ? f32x4{0.f, 0.f, 0.f, 0.f} : st.acc[dt][tt]);
  SBAR();
}
template <int O> __device__ __forceinline__ void g2_own(const G2S& st, f32x4& b0, f32x4& b1) { b0 = st.acc[2 * (O & 1)][O >> 1]; b1 = st.acc[2 * (O & 1) + 1][O >> 1]; }
__device__ __forceinline__ void g2_scatter(const G2S& st, unsigned w, int lane) {
#pragma unroll
  for (int tt = 0; tt < 4; ++tt)
#pragma unroll
    for (int dt = 0; dt < 4; ++dt) {
      const unsigned o = 2 * tt + (dt >> 1);
      const unsigned src = (w - o - 1) & 7;
      const unsigned a = o == w ? G2_END + (dt & 1) * 1024 : G2_RED + (o * 7 + src) * 2048 + (dt & 1) * 1024;
      if (o != w) *(LAS f32x4*)(uintptr_t)(a + lane * 16) = st.acc[dt][tt];
    }
}
__device__ __forceinline__ void g2_finish(const Params& p, const G2S& st, int t0, int ct, unsigned w, int lane, float gt) {
  f32x4 b0, b1;
  switch (w) {
    case 0: g2_own<0>(st, b0, b1); break; case 1: g2_own<1>(st, b0, b1); break; case 2: g2_own<2>(st, b0, b1); break; case 3: g2_own<3>(st, b0, b1); break;
    case 4: g2_own<4>(st, b0, b1); break; case 5: g2_own<5>(st, b0, b1); break; case 6: g2_own<6>(st, b0, b1); break; default: g2_own<7>(st, b0, b1); break;
  }
#pragma unroll
  for (int s = 0; s < 7; ++s) {
    b0 += *(const LAS f32x4*)(uintptr_t)(G2_RED + (w * 7 + s) * 2048 + lane * 16);
    b1 += *(const LAS f32x4*)(uintptr_t)(G2_RED + (w * 7 + s) * 2048 + 1024 + lane * 16);
  }
  const int n2 = w & 1, tto = w >> 1;
  const int h = lane >> 4, q = (lane & 15) >> 2, pp = lane & 3;
  const unsigned aa = G2_AT + (32 * n2 + 8 * h + q) * G2_AROW + (16 * tto + 4 * pp) * 2;
  const v2u a0 = trread(aa), a1 = trread(aa + 4 * G2_AROW);
  const float g1 = rbf(1.0f - gt);
  float o[8];
#pragma unroll
  for (int v = 0; v < 4; ++v) {
    const float av0 = (v & 1) ? bfhi(a0[v >> 1]) : bflo(a0[v >> 1]);
    const float av1 = (v & 1) ? bfhi(a1[v >> 1]) : bflo(a1[v >> 1]);
    o[v] = rbf(gt * av0) + rbf(g1 * rbf(b0[v]));
    o[4 + v] = rbf(gt * av1) + rbf(g1 * rbf(b1[v]));
  }
  const v4u ov = {cvtpk(o[0], o[1]), cvtpk(o[2], o[3]), cvtpk(o[4], o[5]), cvtpk(o[6], o[7])};
  *(GAS v4u*)((char*)p.out + (size_t)(t0 + 16 * tto + (lane & 15)) * (DM * 2) + (64 * ct + 32 * n2 + 8 * h) * 2) = ov;
}

__global__ void __launch_bounds__(512, 2) k_fused(Params p) {
  const int b = blockIdx.x, xcd = b & 7, slot = b >> 3;
  const int tid = threadIdx.x, lane = tid & 63;
  const unsigned w = rfl(tid >> 6);
  STAMP_AT(p.ws, 0);
  const int ch0 = 2 * b;
  const bool spare = slot >= 28;
  const int panel = 4 * (xcd & 1) + (slot & 3), chunk = 7 * (xcd >> 1) + (slot >> 2);
  unsigned tag;
  KgenIn kin;
  G2W gw;
  ConvIn ci;
  if (spare) {
    const int j = xcd * 4 + (slot - 28);
    tag = __hip_atomic_load((const GAS unsigned*)(p.ws + WS_EPOCH), __ATOMIC_RELAXED, __HIP_MEMORY_SCOPE_AGENT) + 1u;
    pre_phase(p, j);
    kgen_load(p, ch0, w, lane, kin);
    WAIT_VM_LGKM_BAR(7);
    if (tid == 0) flag_set(p.ws, WS_FLAGP + j * FLAG_STRIDE, tag);
    STAMP_AT(p.ws, 1);
    kgen_wait<0>(kin);
    const v4u li = kgen_load_idx();
    kgen_phase(kin, w, lane);
    LGKM_BAR();
    kgen_expand(p, ch0, li);
    STAMP_AT(p.ws, 2);
    flag_wait(flag_peek(p.ws, WS_FLAGP, 32, tag, lane), p.ws, WS_FLAGP, 32, tag, lane);
    conv_load(p, ch0, w, ci);
    WAIT_VM_LGKM_BAR(0);
    conv_dma(p, ch0, w, lane);
    STAMP_AT(p.ws, 3);
    STAMP_AT(p.ws, 4);
  } else {
    G1S st;
    g1_setup(p, st, panel, chunk, w, lane);
    kgen_load(p, ch0, w, lane, kin);
    v4u li;
    {
      const GAS v4u* t = (const GAS v4u*)&g_lidx + (256 - (tid & 255));
      asm volatile("global_load_dwordx4 %0, %1, off" : "=v"(li) : "v"(t) : "memory");
    }
    g1s_dma_stage<0>(st);
    g1s_dma_stage<1>(st);
    asm volatile("s_waitcnt vmcnt(12)" : "+v"(li) :: "memory");
    kgen_wait<12>(kin);
    STAMP_AT(p.ws, 1);
    kgen_phase(kin, w, lane);
    LGKM_BAR();
    kgen_expand(p, ch0, li);
    STAMP_AT(p.ws, 2);
    g1_main(st, w, lane);
    STAMP_AT(p.ws, 3);
    tag = __hip_atomic_load((const GAS unsigned*)(p.ws + WS_EPOCH), __ATOMIC_RELAXED, __HIP_MEMORY_SCOPE_AGENT) + 1u;
    flag_wait(flag_peek(p.ws, WS_FLAGP, 32, tag, lane), p.ws, WS_FLAGP, 32, tag, lane);
    conv_load(p, ch0, w, ci);
    LGKM_BAR();
    conv_dma(p, ch0, w, lane);
    g1_glu(p, st, panel, chunk, w, lane);
    STAMP_AT(p.ws, 4);
  }
  conv_fill(ci);
  WAIT_VM_LGKM_BAR(0);
  if (!spare && tid == 0) flag_set(p.ws, WS_FLAGH + b * FLAG_STRIDE, tag);
  STAMP_AT(p.ws, 5);
  const int ct = slot & 7, t0 = 256 * xcd + 64 * (slot >> 3);
  g2_setup_w(p, ct, w, lane, gw);
  ConvAcc ca;
  conv_main(w, lane, ca, gw);
  LGKM_BAR();
  STAMP_AT(p.ws, 6);
  conv_finish(p, ch0, w, lane, ca, ci.dsk);
  {
    const int cj = lane < 28 ? lane : 0;
    const int pb = 8 * (4 * (cj % 7) + (xcd & 3)) + 2 * (cj / 7) + (xcd >> 2);
    unsigned v = tag;
    for (int it = 0; it < SPIN_CAP; ++it) {
      v = __hip_atomic_load((const GAS unsigned*)(p.ws + WS_FLAGH + pb * FLAG_STRIDE), __ATOMIC_RELAXED, __HIP_MEMORY_SCOPE_AGENT);
      if (__all(v == tag)) break;
      __builtin_amdgcn_s_sleep(1);
    }
    __builtin_amdgcn_fence(__ATOMIC_ACQUIRE, "wavefront");
  }
  WAIT_VM(0);
  STAMP_AT(p.ws, 7);
  G2S st2;
  g2_load_h<0, 3>(p, t0, w, lane, st2);
  asm volatile("s_barrier" ::: "memory");
  if (tid == 0) flag_set(p.ws, WS_FLAGA + b * FLAG_STRIDE, tag);
  if (b == 224 && w == 7) {
    for (int it = 0; it < SPIN_CAP; ++it) {
      bool ok = true;
#pragma unroll
      for (int k = 0; k < 4; ++k) {
        const int bb = 64 * k + lane;
        const unsigned off = (bb >> 3) >= 28 ? WS_FLAGP + ((bb & 7) * 4 + ((bb >> 3) - 28)) * FLAG_STRIDE : WS_FLAGH + bb * FLAG_STRIDE;
        ok &= __hip_atomic_load((const GAS unsigned*)(p.ws + off), __ATOMIC_RELAXED, __HIP_MEMORY_SCOPE_AGENT) == tag;
      }
      if (__all(ok)) break;
      __builtin_amdgcn_s_sleep(1);
    }
    if (lane == 0) flag_set(p.ws, WS_EPOCH, tag);
  }
  g2_step<0>(st2, gw);
  g2_load_h<3, 5>(p, t0, w, lane, st2);
  g2_step<1>(st2, gw);
  g2_load_h<5, 7>(p, t0, w, lane, st2);
  g2_step<2>(st2, gw); g2_step<3>(st2, gw); g2_step<4>(st2, gw);
  const unsigned fa = flag_peek(p.ws, WS_FLAGA + 32 * ct * FLAG_STRIDE, 32, tag, lane);
  g2_step<5>(st2, gw);
  flag_wait(fa, p.ws, WS_FLAGA + 32 * ct * FLAG_STRIDE, 32, tag, lane);
  const rsrc_t rsS = mkrsrc(p.ws, WS_DBG);
  const v4u av = bld128(rsS, (unsigned)(WS_AT + (64 * ct + (tid >> 3)) * (TL * 2) + (t0 + 8 * (tid & 7)) * 2), 0, 1);
  const float gt = __uint_as_float(__builtin_amdgcn_raw_buffer_load_b32(rsS, (unsigned)(WS_G + (t0 + 16 * (w >> 1) + (lane & 15)) * 4), 0, AUX_SC1));
  SBAR();
  g2_step<6>(st2, gw);
  STAMP_AT(p.ws, 8);
  g2_scatter(st2, w, lane);
  ldswr128(G2_AT + (tid >> 3) * G2_AROW + (tid & 7) * 16, av);
  LGKM_BAR();
  STAMP_AT(p.ws, 9);
  g2_finish(p, st2, t0, ct, w, lane, gt);
  STAMP_AT(p.ws, 10);
}

extern "C" void kernel_launch(void* const* d_in, const int* in_sizes, int n_in, void* d_out, int out_size, void* d_ws, size_t ws_size, hipStream_t stream) {
  Params p;
  p.x = (const u16*)d_in[0]; p.Are = (const u16*)d_in[1]; p.Aim = (const u16*)d_in[2]; p.Bre = (const u16*)d_in[3]; p.Bim = (const u16*)d_in[4];
  p.Cre = (const u16*)d_in[5]; p.Cim = (const u16*)d_in[6]; p.logdt = (const u16*)d_in[7]; p.Dskip = (const u16*)d_in[8];
  p.wg = (const u16*)d_in[10]; p.wu = (const u16*)d_in[11]; p.wd = (const u16*)d_in[12]; p.gw = (const u16*)d_in[13];
  p.out = (u16*)d_out; p.ws = (unsigned char*)d_ws;
  (void)hipFuncSetAttribute((const void*)k_fused, hipFuncAttributeMaxDynamicSharedMemorySize, 160 * 1024);
  k_fused<<<256, 512, 160 * 1024, stream>>>(p);
}
```

```cpp
#define PAD_US 50
constexpr unsigned G2_RED = 0;
constexpr unsigned G2_AT = 8 * 7 * 2048;
constexpr unsigned G2_AROW = 144;
constexpr unsigned G2_END = G2_AT + 64 * G2_AROW;

struct G2W {
  v4u wf[7][4];
  rsrc_t rs;
  unsigned voff, soff;
  template <int I> __device__ __forceinline__ void load() {
    if constexpr (I < 28) wf[I >> 2][I & 3] = bld128(rs, voff, soff + I * 1024, 1);
  }
  template <int I> __device__ __forceinline__ void at() { load<I>(); }
};
__device__ __forceinline__ void g2_setup_w(const Params& p, int ct, unsigned w, int lane, G2W& g) {
  g.rs = mkrsrc(p.ws, WS_DBG);
  g.voff = (unsigned)(lane * 16);
  g.soff = (unsigned)(WS_WD + (ct * 56 + 7 * w) * 4096);
}
struct G2S { f32x4 acc[4][4]; v4u hf[7][4]; };
template <int LO, int HI> __device__ __forceinline__ void g2_load_h(const Params& p, int t0, unsigned w, int lane, G2S& st) {
  const rsrc_t rsS = mkrsrc(p.ws, WS_DBG);
#pragma unroll
  for (int s = LO; s < HI; ++s)
#pragma unroll
    for (int tt = 0; tt < 4; ++tt) st.hf[s][tt] = bld128(rsS, (unsigned)(lane * 16), (unsigned)(WS_H + ((t0 / 16 + tt) * 56 + 7 * w + s) * 1024), 1);
  SBAR();
}
template <int S> __device__ __forceinline__ void g2_step(G2S& st, const G2W& g) {
#pragma unroll
  for (int tt = 0; tt < 4; ++tt)
#pragma unroll
    for (int dt = 0; dt < 4; ++dt) st.acc[dt][tt] = mfma16(g.wf[S][dt], st.hf[S][tt], S == 0 ? f32x4{0.f, 0.f, 0.f, 0.f} : st.acc[dt][tt]);
  SBAR();
}
template <int O> __device__ __forceinline__ void g2_own(const G2S& st, f32x4& b0, f32x4& b1) { b0 = st.acc[2 * (O & 1)][O >> 1]; b1 = st.acc[2 * (O & 1) + 1][O >> 1]; }
__device__ __forceinline__ void g2_scatter(const G2S& st, unsigned w, int lane) {
#pragma unroll
  for (int tt = 0; tt < 4; ++tt)
#pragma unroll
    for (int dt = 0; dt < 4; ++dt) {
      const unsigned o = 2 * tt + (dt >> 1);
      const unsigned src = (w - o - 1) & 7;
      const unsigned a = o == w ? G2_END + (dt & 1) * 1024 : G2_RED + (o * 7 + src) * 2048 + (dt & 1) * 1024;
      if (o != w) *(LAS f32x4*)(uintptr_t)(a + lane * 16) = st.acc[dt][tt];
    }
}
__device__ __forceinline__ void g2_finish(const Params& p, const G2S& st, int t0, int ct, unsigned w, int lane, float gt) {
  f32x4 b0, b1;
  switch (w) {
    case 0: g2_own<0>(st, b0, b1); break; case 1: g2_own<1>(st, b0, b1); break; case 2: g2_own<2>(st, b0, b1); break; case 3: g2_own<3>(st, b0, b1); break;
    case 4: g2_own<4>(st, b0, b1); break; case 5: g2_own<5>(st, b0, b1); break; case 6: g2_own<6>(st, b0, b1); break; default: g2_own<7>(st, b0, b1); break;
  }
#pragma unroll
  for (int s = 0; s < 7; ++s) {
    b0 += *(const LAS f32x4*)(uintptr_t)(G2_RED + (w * 7 + s) * 2048 + lane * 16);
    b1 += *(const LAS f32x4*)(uintptr_t)(G2_RED + (w * 7 + s) * 2048 + 1024 + lane * 16);
  }
  const int n2 = w & 1, tto = w >> 1;
  const int h = lane >> 4, q = (lane & 15) >> 2, pp = lane & 3;
  const unsigned aa = G2_AT + (32 * n2 + 8 * h + q) * G2_AROW + (16 * tto + 4 * pp) * 2;
  const v2u a0 = trread(aa), a1 = trread(aa + 4 * G2_AROW);
  const float g1 = rbf(1.0f - gt);
  float o[8];
#pragma unroll
  for (int v = 0; v < 4; ++v) {
    const float av0 = (v & 1) ? bfhi(a0[v >> 1]) : bflo(a0[v >> 1]);
    const float av1 = (v & 1) ? bfhi(a1[v >> 1]) : bflo(a1[v >> 1]);
    o[v] = rbf(gt * av0) + rbf(g1 * rbf(b0[v]));
    o[4 + v] = rbf(gt * av1) + rbf(g1 * rbf(b1[v]));
  }
  const v4u ov = {cvtpk(o[0], o[1]), cvtpk(o[2], o[3]), cvtpk(o[4], o[5]), cvtpk(o[6], o[7])};
  *(GAS v4u*)((char*)p.out + (size_t)(t0 + 16 * tto + (lane & 15)) * (DM * 2) + (64 * ct + 32 * n2 + 8 * h) * 2) = ov;
}

__global__ void __launch_bounds__(512, 2) k_fused(Params p) {
  const int b = blockIdx.x, xcd = b & 7, slot = b >> 3;
  const int tid = threadIdx.x, lane = tid & 63;
  const unsigned w = rfl(tid >> 6);
  STAMP_DECL;
  if (blockIdx.x == 0 && threadIdx.x == 0) __hip_atomic_store((GAS unsigned long long*)(p.ws + 2048), pad_r0_, __ATOMIC_RELAXED, __HIP_MEMORY_SCOPE_AGENT);
  STAMP_AT(p.ws, 0);
  const int ch0 = 2 * b;
  const bool spare = slot >= 28;
  const int panel = 4 * (xcd & 1) + (slot & 3), chunk = 7 * (xcd >> 1) + (slot >> 2);
  unsigned tag;
  KgenIn kin;
  G2W gw;
  ConvIn ci;
  if (spare) {
    const int j = xcd * 4 + (slot - 28);
    tag = __hip_atomic_load((const GAS unsigned*)(p.ws + WS_EPOCH), __ATOMIC_RELAXED, __HIP_MEMORY_SCOPE_AGENT) + 1u;
    pre_phase(p, j);
    kgen_load(p, ch0, w, lane, kin);
    WAIT_VM_LGKM_BAR(7);
    if (tid == 0) flag_set(p.ws, WS_FLAGP + j * FLAG_STRIDE, tag);
    STAMP_AT(p.ws, 1);
    kgen_wait<0>(kin);
    const v4u li = kgen_load_idx();
    kgen_phase(kin, w, lane);
    LGKM_BAR();
    kgen_expand(p, ch0, li);
    STAMP_AT(p.ws, 2);
    flag_wait(flag_peek(p.ws, WS_FLAGP, 32, tag, lane), p.ws, WS_FLAGP, 32, tag, lane);
    conv_load(p, ch0, w, ci);
    WAIT_VM_LGKM_BAR(0);
    conv_dma(p, ch0, w, lane);
    STAMP_AT(p.ws, 3);
    STAMP_AT(p.ws, 4);
  } else {
    G1S st;
    g1_setup(p, st, panel, chunk, w, lane);
    kgen_load(p, ch0, w, lane, kin);
    v4u li;
    {
      const GAS v4u* t = (const GAS v4u*)&g_lidx + (256 - (tid & 255));
      asm volatile("global_load_dwordx4 %0, %1, off" : "=v"(li) : "v"(t) : "memory");
    }
    g1s_dma_stage<0>(st);
    g1s_dma_stage<1>(st);
    asm volatile("s_waitcnt vmcnt(12)" : "+v"(li) :: "memory");
    kgen_wait<12>(kin);
    STAMP_AT(p.ws, 1);
    kgen_phase(kin, w, lane);
    LGKM_BAR();
    kgen_expand(p, ch0, li);
    STAMP_AT(p.ws, 2);
    g1_main(st, w, lane);
    STAMP_AT(p.ws, 3);
    tag = __hip_atomic_load((const GAS unsigned*)(p.ws + WS_EPOCH), __ATOMIC_RELAXED, __HIP_MEMORY_SCOPE_AGENT) + 1u;
    flag_wait(flag_peek(p.ws, WS_FLAGP, 32, tag, lane), p.ws, WS_FLAGP, 32, tag, lane);
    conv_load(p, ch0, w, ci);
    LGKM_BAR();
    conv_dma(p, ch0, w, lane);
    g1_glu(p, st, panel, chunk, w, lane);
    STAMP_AT(p.ws, 4);
  }
  conv_fill(ci);
  WAIT_VM_LGKM_BAR(0);
  if (!spare && tid == 0) flag_set(p.ws, WS_FLAGH + b * FLAG_STRIDE, tag);
  STAMP_AT(p.ws, 5);
  const int ct = slot & 7, t0 = 256 * xcd + 64 * (slot >> 3);
  g2_setup_w(p, ct, w, lane, gw);
  ConvAcc ca;
  conv_main(w, lane, ca, gw);
  LGKM_BAR();
  STAMP_AT(p.ws, 6);
  conv_finish(p, ch0, w, lane, ca, ci.dsk);
  {
    const int cj = lane < 28 ? lane : 0;
    const int pb = 8 * (4 * (cj % 7) + (xcd & 3)) + 2 * (cj / 7) + (xcd >> 2);
    unsigned v = tag;
    for (int it = 0; it < SPIN_CAP; ++it) {
      v = __hip_atomic_load((const GAS unsigned*)(p.ws + WS_FLAGH + pb * FLAG_STRIDE), __ATOMIC_RELAXED, __HIP_MEMORY_SCOPE_AGENT);
      if (__all(v == tag)) break;
      __builtin_amdgcn_s_sleep(1);
    }
    __builtin_amdgcn_fence(__ATOMIC_ACQUIRE, "wavefront");
  }
  WAIT_VM(0);
  STAMP_AT(p.ws, 7);
  G2S st2;
  g2_load_h<0, 3>(p, t0, w, lane, st2);
  asm volatile("s_barrier" ::: "memory");
  if (tid == 0) flag_set(p.ws, WS_FLAGA + b * FLAG_STRIDE, tag);
  if (b == 224 && w == 7) {
    for (int it = 0; it < SPIN_CAP; ++it) {
      bool ok = true;
#pragma unroll
      for (int k = 0; k < 4; ++k) {
        const int bb = 64 * k + lane;
        const unsigned off = (bb >> 3) >= 28 ? WS_FLAGP + ((bb & 7) * 4 + ((bb >> 3) - 28)) * FLAG_STRIDE : WS_FLAGH + bb * FLAG_STRIDE;
        ok &= __hip_atomic_load((const GAS unsigned*)(p.ws + off), __ATOMIC_RELAXED, __HIP_MEMORY_SCOPE_AGENT) == tag;
      }
      if (__all(ok)) break;
      __builtin_amdgcn_s_sleep(1);
    }
    if (lane == 0) flag_set(p.ws, WS_EPOCH, tag);
  }
  g2_step<0>(st2, gw);
  g2_load_h<3, 5>(p, t0, w, lane, st2);
  g2_step<1>(st2, gw);
  g2_load_h<5, 7>(p, t0, w, lane, st2);
  g2_step<2>(st2, gw); g2_step<3>(st2, gw); g2_step<4>(st2, gw);
  const unsigned fa = flag_peek(p.ws, WS_FLAGA + 32 * ct * FLAG_STRIDE, 32, tag, lane);
  g2_step<5>(st2, gw);
  flag_wait(fa, p.ws, WS_FLAGA + 32 * ct * FLAG_STRIDE, 32, tag, lane);
  const rsrc_t rsS = mkrsrc(p.ws, WS_DBG);
  const v4u av = bld128(rsS, (unsigned)(WS_AT + (64 * ct + (tid >> 3)) * (TL * 2) + (t0 + 8 * (tid & 7)) * 2), 0, 1);
  const float gt = __uint_as_float(__builtin_amdgcn_raw_buffer_load_b32(rsS, (unsigned)(WS_G + (t0 + 16 * (w >> 1) + (lane & 15)) * 4), 0, AUX_SC1));
  SBAR();
  g2_step<6>(st2, gw);
  STAMP_AT(p.ws, 8);
  g2_scatter(st2, w, lane);
  ldswr128(G2_AT + (tid >> 3) * G2_AROW + (tid & 7) * 16, av);
  LGKM_BAR();
  STAMP_AT(p.ws, 9);
  g2_finish(p, st2, t0, ct, w, lane, gt);
  STAMP_AT(p.ws, 10);
  STAMP_FLUSH(p.ws);
}

extern "C" void kernel_launch(void* const* d_in, const int* in_sizes, int n_in, void* d_out, int out_size, void* d_ws, size_t ws_size, hipStream_t stream) {
  Params p;
  p.x = (const u16*)d_in[0]; p.Are = (const u16*)d_in[1]; p.Aim = (const u16*)d_in[2]; p.Bre = (const u16*)d_in[3]; p.Bim = (const u16*)d_in[4];
  p.Cre = (const u16*)d_in[5]; p.Cim = (const u16*)d_in[6]; p.logdt = (const u16*)d_in[7]; p.Dskip = (const u16*)d_in[8];
  p.wg = (const u16*)d_in[10]; p.wu = (const u16*)d_in[11]; p.wd = (const u16*)d_in[12]; p.gw = (const u16*)d_in[13];
  p.out = (u16*)d_out; p.ws = (unsigned char*)d_ws;
  (void)hipFuncSetAttribute((const void*)k_fused, hipFuncAttributeMaxDynamicSharedMemorySize, 160 * 1024);
  k_fused<<<256, 512, 160 * 1024, stream>>>(p);
}
```

```cpp
constexpr unsigned G2_RED = 0;
constexpr unsigned G2_AT = 8 * 7 * 2048;
constexpr unsigned G2_AROW = 144;
constexpr unsigned G2_END = G2_AT + 64 * G2_AROW;

struct G2W {
  v4u wf[7][4];
  rsrc_t rs;
  unsigned voff, soff;
  template <int I> __device__ __forceinline__ void load() {
    if constexpr (I < 28) wf[I >> 2][I & 3] = bld128(rs, voff, soff + I * 1024, 1);
  }
  template <int I> __device__ __forceinline__ void at() { load<I>(); }
};
__device__ __forceinline__ void g2_setup_w(const Params& p, int ct, unsigned w, int lane, G2W& g) {
  g.rs = mkrsrc(p.ws, WS_DBG);
  g.voff = (unsigned)(lane * 16);
  g.soff = (unsigned)(WS_WD + (ct * 56 + 7 * w) * 4096);
}
struct G2S { f32x4 acc[4][4]; v4u hf[7][4]; };
template <int LO, int HI> __device__ __forceinline__ void g2_load_h(const Params& p, int t0, unsigned w, int lane, G2S& st) {
  const rsrc_t rsS = mkrsrc(p.ws, WS_DBG);
#pragma unroll
  for (int s = LO; s < HI; ++s)
#pragma unroll
    for (int tt = 0; tt < 4; ++tt) st.hf[s][tt] = bld128(rsS, (unsigned)(lane * 16), (unsigned)(WS_H + ((t0 / 16 + tt) * 56 + 7 * w + s) * 1024), 1);
  SBAR();
}
template <int S> __device__ __forceinline__ void g2_step(G2S& st, const G2W& g) {
#pragma unroll
  for (int tt = 0; tt < 4; ++tt)
#pragma unroll
    for (int dt = 0; dt < 4; ++dt) st.acc[dt][tt] = mfma16(g.wf[S][dt], st.hf[S][tt], S == 0 ? f32x4{0.f, 0.f, 0.f, 0.f} : st.acc[dt][tt]);
  SBAR();
}
template <int O> __device__ __forceinline__ void g2_own(const G2S& st, f32x4& b0, f32x4& b1) { b0 = st.acc[2 * (O & 1)][O >> 1]; b1 = st.acc[2 * (O & 1) + 1][O >> 1]; }
__device__ __forceinline__ void g2_scatter(const G2S& st, unsigned w, int lane) {
#pragma unroll
  for (int tt = 0; tt < 4; ++tt)
#pragma unroll
    for (int dt = 0; dt < 4; ++dt) {
      const unsigned o = 2 * tt + (dt >> 1);
      const unsigned src = (w - o - 1) & 7;
      const unsigned a = o == w ? G2_END + (dt & 1) * 1024 : G2_RED + (o * 7 + src) * 2048 + (dt & 1) * 1024;
      if (o != w) *(LAS f32x4*)(uintptr_t)(a + lane * 16) = st.acc[dt][tt];
    }
}
__device__ __forceinline__ void g2_finish(const Params& p, const G2S& st, int t0, int ct, unsigned w, int lane, float gt) {
  f32x4 b0, b1;
  switch (w) {
    case 0: g2_own<0>(st, b0, b1); break; case 1: g2_own<1>(st, b0, b1); break; case 2: g2_own<2>(st, b0, b1); break; case 3: g2_own<3>(st, b0, b1); break;
    case 4: g2_own<4>(st, b0, b1); break; case 5: g2_own<5>(st, b0, b1); break; case 6: g2_own<6>(st, b0, b1); break; default: g2_own<7>(st, b0, b1); break;
  }
#pragma unroll
  for (int s = 0; s < 7; ++s) {
    b0 += *(const LAS f32x4*)(uintptr_t)(G2_RED + (w * 7 + s) * 2048 + lane * 16);
    b1 += *(const LAS f32x4*)(uintptr_t)(G2_RED + (w * 7 + s) * 2048 + 1024 + lane * 16);
  }
  const int n2 = w & 1, tto = w >> 1;
  const int h = lane >> 4, q = (lane & 15) >> 2, pp = lane & 3;
  const unsigned aa = G2_AT + (32 * n2 + 8 * h + q) * G2_AROW + (16 * tto + 4 * pp) * 2;
  const v2u a0 = trread(aa), a1 = trread(aa + 4 * G2_AROW);
  const float g1 = rbf(1.0f - gt);
  float o[8];
#pragma unroll
  for (int v = 0; v < 4; ++v) {
    const float av0 = (v & 1) ? bfhi(a0[v >> 1]) : bflo(a0[v >> 1]);
    const float av1 = (v & 1) ? bfhi(a1[v >> 1]) : bflo(a1[v >> 1]);
    o[v] = rbf(gt * av0) + rbf(g1 * rbf(b0[v]));
    o[4 + v] = rbf(gt * av1) + rbf(g1 * rbf(b1[v]));
  }
  const v4u ov = {cvtpk(o[0], o[1]), cvtpk(o[2], o[3]), cvtpk(o[4], o[5]), cvtpk(o[6], o[7])};
  *(GAS v4u*)((char*)p.out + (size_t)(t0 + 16 * tto + (lane & 15)) * (DM * 2) + (64 * ct + 32 * n2 + 8 * h) * 2) = ov;
}

__global__ void __launch_bounds__(512, 2) k_fused(Params p) {
  const int b = blockIdx.x, xcd = b & 7, slot = b >> 3;
  const int tid = threadIdx.x, lane = tid & 63;
  const unsigned w = rfl(tid >> 6);
  if (blockIdx.x == 0 && threadIdx.x == 0) __hip_atomic_store((GAS unsigned long long*)(p.ws + 2048), __builtin_amdgcn_s_memrealtime(), __ATOMIC_RELAXED, __HIP_MEMORY_SCOPE_AGENT);
  STAMP_AT(p.ws, 0);
  const int ch0 = 2 * b;
  const bool spare = slot >= 28;
  const int panel = 4 * (xcd & 1) + (slot & 3), chunk = 7 * (xcd >> 1) + (slot >> 2);
  unsigned tag;
  KgenIn kin;
  G2W gw;
  ConvIn ci;
  if (spare) {
    const int j = xcd * 4 + (slot - 28);
    tag = __hip_atomic_load((const GAS unsigned*)(p.ws + WS_EPOCH), __ATOMIC_RELAXED, __HIP_MEMORY_SCOPE_AGENT) + 1u;
    pre_phase(p, j);
    kgen_load(p, ch0, w, lane, kin);
    WAIT_VM_LGKM_BAR(7);
    if (tid == 0) flag_set(p.ws, WS_FLAGP + j * FLAG_STRIDE, tag);
    STAMP_AT(p.ws, 1);
    kgen_wait<0>(kin);
    const v4u li = kgen_load_idx();
    kgen_phase(kin, w, lane);
    LGKM_BAR();
    kgen_expand(p, ch0, li);
    STAMP_AT(p.ws, 2);
    flag_wait(flag_peek(p.ws, WS_FLAGP, 32, tag, lane), p.ws, WS_FLAGP, 32, tag, lane);
    conv_load(p, ch0, w, ci);
    WAIT_VM_LGKM_BAR(0);
    conv_dma(p, ch0, w, lane);
    STAMP_AT(p.ws, 3);
    STAMP_AT(p.ws, 4);
  } else {
    G1S st;
    g1_setup(p, st, panel, chunk, w, lane);
    kgen_load(p, ch0, w, lane, kin);
    v4u li;
    {
      const GAS v4u* t = (const GAS v4u*)&g_lidx + (256 - (tid & 255));
      asm volatile("global_load_dwordx4 %0, %1, off" : "=v"(li) : "v"(t) : "memory");
    }
    g1s_dma_stage<0>(st);
    g1s_dma_stage<1>(st);
    asm volatile("s_waitcnt vmcnt(12)" : "+v"(li) :: "memory");
    kgen_wait<12>(kin);
    STAMP_AT(p.ws, 1);
    kgen_phase(kin, w, lane);
    LGKM_BAR();
    kgen_expand(p, ch0, li);
    STAMP_AT(p.ws, 2);
    g1_main(st, w, lane);
    STAMP_AT(p.ws, 3);
    tag = __hip_atomic_load((const GAS unsigned*)(p.ws + WS_EPOCH), __ATOMIC_RELAXED, __HIP_MEMORY_SCOPE_AGENT) + 1u;
    flag_wait(flag_peek(p.ws, WS_FLAGP, 32, tag, lane), p.ws, WS_FLAGP, 32, tag, lane);
    conv_load(p, ch0, w, ci);
    LGKM_BAR();
    conv_dma(p, ch0, w, lane);
    g1_glu(p, st, panel, chunk, w, lane);
    STAMP_AT(p.ws, 4);
  }
  conv_fill(ci);
  WAIT_VM_LGKM_BAR(0);
  if (!spare && tid == 0) flag_set(p.ws, WS_FLAGH + b * FLAG_STRIDE, tag);
  STAMP_AT(p.ws, 5);
  const int ct = slot & 7, t0 = 256 * xcd + 64 * (slot >> 3);
  g2_setup_w(p, ct, w, lane, gw);
  ConvAcc ca;
  conv_main(w, lane, ca, gw);
  LGKM_BAR();
  STAMP_AT(p.ws, 6);
  conv_finish(p, ch0, w, lane, ca, ci.dsk);
  {
    const int cj = lane < 28 ? lane : 0;
    const int pb = 8 * (4 * (cj % 7) + (xcd & 3)) + 2 * (cj / 7) + (xcd >> 2);
    unsigned v = tag;
    for (int it = 0; it < SPIN_CAP; ++it) {
      v = __hip_atomic_load((const GAS unsigned*)(p.ws + WS_FLAGH + pb * FLAG_STRIDE), __ATOMIC_RELAXED, __HIP_MEMORY_SCOPE_AGENT);
      if (__all(v == tag)) break;
      __builtin_amdgcn_s_sleep(1);
    }
    __builtin_amdgcn_fence(__ATOMIC_ACQUIRE, "wavefront");
  }
  WAIT_VM(0);
  STAMP_AT(p.ws, 7);
  G2S st2;
  g2_load_h<0, 3>(p, t0, w, lane, st2);
  asm volatile("s_barrier" ::: "memory");
  if (tid == 0) flag_set(p.ws, WS_FLAGA + b * FLAG_STRIDE, tag);
  if (b == 224 && w == 7) {
    for (int it = 0; it < SPIN_CAP; ++it) {
      bool ok = true;
#pragma unroll
      for (int k = 0; k < 4; ++k) {
        const int bb = 64 * k + lane;
        const unsigned off = (bb >> 3) >= 28 ? WS_FLAGP + ((bb & 7) * 4 + ((bb >> 3) - 28)) * FLAG_STRIDE : WS_FLAGH + bb * FLAG_STRIDE;
        ok &= __hip_atomic_load((const GAS unsigned*)(p.ws + off), __ATOMIC_RELAXED, __HIP_MEMORY_SCOPE_AGENT) == tag;
      }
      if (__all(ok)) break;
      __builtin_amdgcn_s_sleep(1);
    }
    if (lane == 0) flag_set(p.ws, WS_EPOCH, tag);
  }
  g2_step<0>(st2, gw);
  g2_load_h<3, 5>(p, t0, w, lane, st2);
  g2_step<1>(st2, gw);
  g2_load_h<5, 7>(p, t0, w, lane, st2);
  g2_step<2>(st2, gw); g2_step<3>(st2, gw); g2_step<4>(st2, gw);
  const unsigned fa = flag_peek(p.ws, WS_FLAGA + 32 * ct * FLAG_STRIDE, 32, tag, lane);
  g2_step<5>(st2, gw);
  flag_wait(fa, p.ws, WS_FLAGA + 32 * ct * FLAG_STRIDE, 32, tag, lane);
  const rsrc_t rsS = mkrsrc(p.ws, WS_DBG);
  const v4u av = bld128(rsS, (unsigned)(WS_AT + (64 * ct + (tid >> 3)) * (TL * 2) + (t0 + 8 * (tid & 7)) * 2), 0, 1);
  const float gt = __uint_as_float(__builtin_amdgcn_raw_buffer_load_b32(rsS, (unsigned)(WS_G + (t0 + 16 * (w >> 1) + (lane & 15)) * 4), 0, AUX_SC1));
  SBAR();
  g2_step<6>(st2, gw);
  STAMP_AT(p.ws, 8);
  g2_scatter(st2, w, lane);
  ldswr128(G2_AT + (tid >> 3) * G2_AROW + (tid & 7) * 16, av);
  LGKM_BAR();
  STAMP_AT(p.ws, 9);
  g2_finish(p, st2, t0, ct, w, lane, gt);
  STAMP_AT(p.ws, 10);
  STAMP_FLUSH(p.ws);
}

__global__ void k_tiny(unsigned char* ws) {
  const unsigned long long tgt = __hip_atomic_load((const GAS unsigned long long*)(ws + 2048), __ATOMIC_RELAXED, __HIP_MEMORY_SCOPE_AGENT) + 5000;
  for (int i = 0; i < 100000 && __builtin_amdgcn_s_memrealtime() < tgt; ++i) __builtin_amdgcn_s_sleep(2);
}
extern "C" void kernel_launch(void* const* d_in, const int* in_sizes, int n_in, void* d_out, int out_size, void* d_ws, size_t ws_size, hipStream_t stream) {
  Params p;
  p.x = (const u16*)d_in[0]; p.Are = (const u16*)d_in[1]; p.Aim = (const u16*)d_in[2]; p.Bre = (const u16*)d_in[3]; p.Bim = (const u16*)d_in[4];
  p.Cre = (const u16*)d_in[5]; p.Cim = (const u16*)d_in[6]; p.logdt = (const u16*)d_in[7]; p.Dskip = (const u16*)d_in[8];
  p.wg = (const u16*)d_in[10]; p.wu = (const u16*)d_in[11]; p.wd = (const u16*)d_in[12]; p.gw = (const u16*)d_in[13];
  p.out = (u16*)d_out; p.ws = (unsigned char*)d_ws;
  (void)hipFuncSetAttribute((const void*)k_fused, hipFuncAttributeMaxDynamicSharedMemorySize, 160 * 1024);
  k_fused<<<256, 512, 160 * 1024, stream>>>(p);
  k_tiny<<<1, 64, 0, stream>>>(p.ws);
}
```

```cpp
#include <hip/hip_runtime.h>
#include <stdint.h>

#define GAS __attribute__((address_space(1)))
#define LAS __attribute__((address_space(3)))
typedef unsigned short u16;
typedef unsigned v4u __attribute__((ext_vector_type(4)));
typedef unsigned v2u __attribute__((ext_vector_type(2)));
typedef float f32x4 __attribute__((ext_vector_type(4)));
typedef float f32x2 __attribute__((ext_vector_type(2)));
typedef short bf16x8 __attribute__((ext_vector_type(8)));
typedef short v4i16_t __attribute__((ext_vector_type(4)));
typedef __amdgpu_buffer_rsrc_t rsrc_t;

constexpr int TL = 2048, DM = 512, NM = 128, FF = 1792;
constexpr int AUX_SC1 = 16;
constexpr int RSRC_W3 = 0x00020000;

constexpr unsigned WS_EPOCH = 0;
constexpr unsigned WS_FLAGP = 4096;
constexpr unsigned WS_FLAGH = 8192;
constexpr unsigned WS_FLAGW = 8192 + 32768;
constexpr unsigned WS_G     = 131072;
constexpr unsigned TB_PITCH = DM * 128 + 256;
constexpr unsigned WS_UT    = 262144;
constexpr unsigned AT_PITCH = DM * 256 + 256;
constexpr unsigned A_TAG2   = 0x9e3779b9u;
constexpr unsigned WS_AT    = WS_UT + 32 * TB_PITCH;
constexpr unsigned WS_WD    = WS_AT + 32 * AT_PITCH;
constexpr unsigned WS_H     = WS_WD + FF * DM * 2;
constexpr unsigned WS_END   = WS_H + TL * FF * 2;

struct Params {
  const u16 *x, *Are, *Aim, *Bre, *Bim, *Cre, *Cim, *logdt, *Dskip, *wg, *wu, *wd, *gw;
  u16* out;
  unsigned char* ws;
};

__device__ __forceinline__ float bflo(unsigned v) { return __uint_as_float(v << 16); }
__device__ __forceinline__ float bfhi(unsigned v) { return __uint_as_float(v & 0xffff0000u); }
__device__ __forceinline__ unsigned cvtpk(float lo, float hi) {
  typedef __bf16 bf2 __attribute__((ext_vector_type(2)));
  f32x2 v = {lo, hi};
  bf2 r = __builtin_convertvector(v, bf2);
  return __builtin_bit_cast(unsigned, r);
}
__device__ __forceinline__ float rbf(float f) { return bflo(cvtpk(f, f)); }
__device__ __forceinline__ f32x4 mfma16(v4u a, v4u b, f32x4 c) {
  return __builtin_amdgcn_mfma_f32_16x16x32_bf16(__builtin_bit_cast(bf16x8, a), __builtin_bit_cast(bf16x8, b), c, 0, 0, 0);
}
__device__ __forceinline__ v2u trread(unsigned addr) {
  return __builtin_bit_cast(v2u, __builtin_amdgcn_ds_read_tr16_b64_v4i16((LAS v4i16_t*)(uintptr_t)addr));
}
__device__ __forceinline__ v4u ldsrd128(unsigned addr) { return *(const LAS v4u*)(uintptr_t)addr; }
__device__ __forceinline__ v2u ldsrd64(unsigned addr) { return *(const LAS v2u*)(uintptr_t)addr; }
__device__ __forceinline__ float ldsrdf(unsigned addr) { return *(const LAS float*)(uintptr_t)addr; }
__device__ __forceinline__ void ldswr128(unsigned addr, v4u v) { *(LAS v4u*)(uintptr_t)addr = v; }
__device__ __forceinline__ void ldswrf(unsigned addr, float v) { *(LAS float*)(uintptr_t)addr = v; }
__device__ __forceinline__ unsigned rfl(unsigned v) { return __builtin_amdgcn_readfirstlane(v); }
__device__ __forceinline__ rsrc_t mkrsrc(const void* p, unsigned bytes) { return __builtin_amdgcn_make_buffer_rsrc((void*)p, 0, bytes, RSRC_W3); }
__device__ __forceinline__ v4u bld128(rsrc_t rs, unsigned voff, unsigned soff, int aux) { return aux ? __builtin_amdgcn_raw_buffer_load_b128(rs, voff, soff, AUX_SC1) : __builtin_amdgcn_raw_buffer_load_b128(rs, voff, soff, 0); }
__device__ __forceinline__ void glds(unsigned m0v, unsigned voff, rsrc_t rs, unsigned soff) {
  asm volatile("s_mov_b32 m0, %0\n\ts_nop 0\n\tbuffer_load_dwordx4 %1, %2, %3 offen lds" ::"s"(m0v), "v"(voff), "s"(rs), "s"(soff) : "memory");
}
__device__ __forceinline__ void touch(const char* a) {
  asm volatile("s_mov_b32 m0, %0\n\ts_nop 0\n\tglobal_load_lds_dword %1, off" ::"s"(163840 - 256), "v"(a) : "memory");
}
#define WAIT_VM(n) asm volatile("s_waitcnt vmcnt(" #n ")" ::: "memory")
#define WAIT_VM_LGKM_BAR(n) asm volatile("s_waitcnt vmcnt(" #n ") lgkmcnt(0)\n\ts_barrier" ::: "memory")
#define LGKM_BAR() asm volatile("s_waitcnt lgkmcnt(0)\n\ts_barrier" ::: "memory")
#define SBAR() __builtin_amdgcn_sched_barrier(0)
constexpr unsigned FLAG_STRIDE = 128;
constexpr int SPIN_CAP = 1 << 15;
__device__ __forceinline__ void flag_set(unsigned char* ws, unsigned off, unsigned tag) {
  __hip_atomic_store((GAS unsigned*)(ws + off), tag, __ATOMIC_RELAXED, __HIP_MEMORY_SCOPE_AGENT);
}
constexpr unsigned PRE_XROW = 1024 + 32;

__device__ __forceinline__ float wave_sum8(float (&v)[8]) {
  float a[4], b[2];
#pragma unroll
  for (int i = 0; i < 4; ++i) {
    const auto r = __builtin_amdgcn_permlane32_swap(__float_as_uint(v[i]), __float_as_uint(v[i + 4]), false, false);
    a[i] = __uint_as_float(r[0]) + __uint_as_float(r[1]);
  }
#pragma unroll
  for (int i = 0; i < 2; ++i) {
    const auto r = __builtin_amdgcn_permlane16_swap(__float_as_uint(a[i]), __float_as_uint(a[i + 2]), false, false);
    b[i] = __uint_as_float(r[0]) + __uint_as_float(r[1]);
  }
  const float m0 = __uint_as_float(__builtin_amdgcn_update_dpp(0u, __float_as_uint(b[0]), 0x128, 0xf, 0xf, true));
  const float m1 = __uint_as_float(__builtin_amdgcn_update_dpp(0u, __float_as_uint(b[1]), 0x128, 0xf, 0xf, true));
  const int lane = threadIdx.x & 63;
  float c = (lane & 8) ? b[1] + m1 : b[0] + m0;
  c += __uint_as_float(__builtin_amdgcn_update_dpp(0u, __float_as_uint(c), 0x141, 0xf, 0xf, true));
  c += __uint_as_float(__builtin_amdgcn_update_dpp(0u, __float_as_uint(c), 0x4e, 0xf, 0xf, true));
  c += __uint_as_float(__builtin_amdgcn_update_dpp(0u, __float_as_uint(c), 0xb1, 0xf, 0xf, true));
  return c;
}

__device__ __forceinline__ void pre_phase(const Params& p, int j) {
  const int tid = threadIdx.x, lane = tid & 63;
  const unsigned w = rfl(tid >> 6);
  const rsrc_t rsX = mkrsrc(p.x, TL * DM * 2), rsS = mkrsrc(p.ws, WS_END);
  v4u xr[8];
  const v4u gwv = *(const GAS v4u*)((const char*)p.gw + lane * 16);
#pragma unroll
  for (int i = 0; i < 8; ++i) xr[i] = bld128(rsX, (unsigned)((64 * j + 8 * w + i) * 1024 + lane * 16), 0, 0);
  SBAR();
#pragma unroll
  for (int i = 0; i < 8; ++i) ldswr128((8 * w + i) * PRE_XROW + lane * 16, xr[i]);
  {
    float sv[8];
#pragma unroll
    for (int i = 0; i < 8; ++i) {
      float s = 0.f;
#pragma unroll
      for (int c = 0; c < 4; ++c) {
        const unsigned pr = cvtpk(bflo(xr[i][c]) * bflo(gwv[c]), bfhi(xr[i][c]) * bfhi(gwv[c]));
        s += bflo(pr) + bfhi(pr);
      }
      sv[i] = s;
    }
    const float s = rbf(wave_sum8(sv));
    const float gt = rbf(__builtin_amdgcn_rcpf(1.0f + __builtin_amdgcn_exp2f(s * -1.4426950408889634f)));
    const int r = lane >> 3;
    if ((lane & 7) == 0) __hip_atomic_store((GAS unsigned*)(p.ws + WS_G) + 64 * j + 8 * w + r, __float_as_uint(gt), __ATOMIC_RELAXED, __HIP_MEMORY_SCOPE_AGENT);
  }
  LGKM_BAR();
  const int g = lane >> 4, q = (lane & 15) >> 2, pp = lane & 3;
  const bool hi8 = lane & 8;
#pragma unroll
  for (int i = 0; i < 4; ++i) {
    const int d0 = 16 * (4 * w + i);
    const unsigned a = (unsigned)((8 * g + q) * PRE_XROW + (d0 + 4 * pp) * 2);
    const v2u l0 = trread(a), h0 = trread(a + 4 * PRE_XROW), l1 = trread(a + 32 * PRE_XROW), h1 = trread(a + 36 * PRE_XROW);
    const unsigned v0[4] = {l0.x, l0.y, h0.x, h0.y}, v1[4] = {l1.x, l1.y, h1.x, h1.y};
    v4u A, B;
#pragma unroll
    for (int k = 0; k < 4; ++k) {
      const unsigned r0 = __builtin_amdgcn_update_dpp(0u, v0[k], 0x128, 0xf, 0xf, true), r1 = __builtin_amdgcn_update_dpp(0u, v1[k], 0x128, 0xf, 0xf, true);
      A[k] = hi8 ? r1 : v0[k];
      B[k] = hi8 ? v1[k] : r0;
    }
    const unsigned o = WS_UT + j * TB_PITCH + (d0 + (lane & 7)) * 128 + ((hi8 ? 32 : 0) + 8 * g) * 2;
    __builtin_amdgcn_raw_buffer_store_b128(A, rsS, (int)o, 0, AUX_SC1);
    __builtin_amdgcn_raw_buffer_store_b128(B, rsS, (int)(o + 8 * 128), 0, AUX_SC1);
  }
}
constexpr unsigned G1_STAGE = 49152, G1_WOFF = 32768;
typedef float f32x16 __attribute__((ext_vector_type(16)));
__device__ __forceinline__ f32x16 mfma32(v4u a, v4u b, f32x16 c) {
  return __builtin_amdgcn_mfma_f32_32x32x16_bf16(__builtin_bit_cast(bf16x8, a), __builtin_bit_cast(bf16x8, b), c, 0, 0, 0);
}
struct G1S {
  f32x16 acc[2][2];
  v4u xf[2][2], wf[2][2];
  unsigned xa[3][4], wa[3][2];
  unsigned vx[2];
  const char* wsrc[2];
  rsrc_t rsX;
  unsigned w;
  unsigned rot;
};
template <int S, int J> __device__ __forceinline__ void g1s_dma_for(G1S& st, unsigned wp) {
  constexpr unsigned sb = (unsigned)(S % 3) * G1_STAGE;
  if constexpr (J < 4) {
    glds(sb + (32 * wp + 8 * J) * 128, st.vx[J & 1], st.rsX, ((S + st.rot) & 7) * 128 + (unsigned)((J >> 1) * 16 * (DM * 2)) + 32 * wp * (DM * 2));
  } else {
    const char* src = st.wsrc[J - 4] + (size_t)((((S + st.rot) & 7) * 64 + 8 * wp) * (FF * 2));
    asm volatile("s_mov_b32 m0, %0\n\ts_nop 0\n\tglobal_load_lds_dwordx4 %1, off" ::"s"(sb + G1_WOFF + (8 * wp + 4 * (J - 4)) * 256), "v"(src) : "memory");
  }
}
template <int S, int J> __device__ __forceinline__ void g1s_dma(G1S& st) { g1s_dma_for<S, J>(st, st.w); }
template <int S> __device__ __forceinline__ void g1s_dma_stage_for(G1S& st, unsigned wp) {
  g1s_dma_for<S, 0>(st, wp); g1s_dma_for<S, 4>(st, wp); g1s_dma_for<S, 1>(st, wp); g1s_dma_for<S, 5>(st, wp); g1s_dma_for<S, 2>(st, wp); g1s_dma_for<S, 3>(st, wp);
}
template <int KI, int R> __device__ __forceinline__ void g1s_read(G1S& st) {
  constexpr int s = KI >> 2, k4 = KI & 3, b = KI & 1;
  if constexpr (R < 4) {
    const v2u a = trread(st.wa[s % 3][R >> 1] + k4 * 4096 + (R & 1) * 1024);
    if constexpr (R & 1) { st.wf[b][R >> 1].z = a.x; st.wf[b][R >> 1].w = a.y; } else { st.wf[b][R >> 1].x = a.x; st.wf[b][R >> 1].y = a.y; }
  } else {
    st.xf[b][R - 4] = ldsrd128(st.xa[s % 3][k4] + (R - 4) * 4096);
  }
}
template <int KI, class H> __device__ __forceinline__ void g1s_step(G1S& st, H& hook) {
  constexpr int s = KI >> 2, k4 = KI & 3, b = KI & 1;
  constexpr bool rd = KI + 1 < 32;
  hook.template at<KI>();
  if constexpr (rd) { g1s_read<KI + 1, 0>(st); g1s_read<KI + 1, 1>(st); }
  st.acc[0][0] = mfma32(st.wf[b][0], st.xf[b][0], KI == 0 ? f32x16{} : st.acc[0][0]);
  SBAR();
  if constexpr (rd) { g1s_read<KI + 1, 4>(st); g1s_read<KI + 1, 2>(st); }
  if constexpr (k4 == 3 && s + 3 < 8) g1s_dma<s + 3, 0>(st);
  if constexpr (k4 == 0 && s >= 1 && s + 2 < 8) g1s_dma<s + 2, 2>(st);
  if constexpr ((k4 == 1 || k4 == 2) && s >= 1 && s + 2 < 8) g1s_dma<s + 2, 3 + k4>(st);
  st.acc[1][0] = mfma32(st.wf[b][1], st.xf[b][0], KI == 0 ? f32x16{} : st.acc[1][0]);
  SBAR();
  if constexpr (rd) { g1s_read<KI + 1, 3>(st); g1s_read<KI + 1, 5>(st); }
  st.acc[0][1] = mfma32(st.wf[b][0], st.xf[b][1], KI == 0 ? f32x16{} : st.acc[0][1]);
  SBAR();
  if constexpr (k4 == 3 && s + 3 < 8) g1s_dma<s + 3, 1>(st);
  if constexpr (k4 == 0 && s >= 1 && s + 2 < 8) g1s_dma<s + 2, 3>(st);
  st.acc[1][1] = mfma32(st.wf[b][1], st.xf[b][1], KI == 0 ? f32x16{} : st.acc[1][1]);
  SBAR();
  if constexpr (k4 == 2 && s < 7) {
    asm volatile("s_waitcnt vmcnt(%0) lgkmcnt(0)\n\ts_barrier" ::"i"((s < 6 ? 6 : 0) + H::younger(s)) : "memory");
    SBAR();
  }
  if constexpr (KI + 1 < 32) g1s_step<KI + 1>(st, hook);
}
__device__ __forceinline__ void g1_setup(const Params& p, G1S& st, int panel, int chunk, unsigned w, int lane) {
  st.w = w;
  st.rot = (unsigned)(chunk % 7 + 2 * (panel & 3)) & 7;
  st.rsX = mkrsrc(p.x, TL * DM * 2);
#pragma unroll
  for (int j = 0; j < 2; ++j) {
    const int r = 8 * j + (lane >> 3);
    const int c = (lane & 7) ^ ((r >> 1) & 7);
    st.vx[j] = (unsigned)((panel * 256 + r) * (DM * 2) + 16 * c);
  }
#pragma unroll
  for (int j = 0; j < 2; ++j) {
    const int k = 4 * j + (lane >> 4);
    const int pc = lane & 15;
    const int u = (pc >> 2) ^ (k & 3);
    const char* base = (const char*)(u < 2 ? p.wg : p.wu);
    st.wsrc[j] = base + (size_t)k * (FF * 2) + chunk * 128 + (u & 1) * 64 + (pc & 3) * 16;
  }
  const int wm = w >> 1, nh = w & 1;
  const int t = lane & 31, kh = lane >> 5;
  const int blk = (lane >> 4) & 1, q = (lane & 15) >> 2, pp = lane & 3;
#pragma unroll
  for (int b = 0; b < 3; ++b) {
#pragma unroll
    for (int k4 = 0; k4 < 4; ++k4) st.xa[b][k4] = (unsigned)(b * G1_STAGE + (64 * wm + t) * 128 + 16 * ((2 * k4 + kh) ^ ((t >> 1) & 7)));
#pragma unroll
    for (int a = 0; a < 2; ++a)
      st.wa[b][a] = (unsigned)(b * G1_STAGE + G1_WOFF + (8 * kh + q) * 256 + 64 * ((2 * a + nh) ^ q) + 32 * blk + 8 * pp);
  }
}
template <class H>
__device__ __forceinline__ void g1_main(G1S& st, unsigned w, int lane, H& hook) {
  if (w >= 4) WAIT_VM(13);
  LGKM_BAR();
  g1s_read<0, 0>(st); g1s_read<0, 1>(st); g1s_read<0, 4>(st); g1s_read<0, 2>(st); g1s_read<0, 3>(st); g1s_read<0, 5>(st);
  SBAR();
  g1s_step<0>(st, hook);
}
__device__ __forceinline__ v4u g1_glu_tile(const G1S& st, int i) {
  const int tt = i >> 1, h2 = i & 1;
  unsigned hp[4];
#pragma unroll
  for (int j = 0; j < 8; j += 2) {
    const f32x2 gg = {st.acc[0][tt][8 * h2 + j], st.acc[0][tt][8 * h2 + j + 1]}, uu = {st.acc[1][tt][8 * h2 + j], st.acc[1][tt][8 * h2 + j + 1]};
    const f32x2 t = gg * -1.4426950408889634f;
    const f32x2 d = f32x2{__builtin_amdgcn_exp2f(t.x), __builtin_amdgcn_exp2f(t.y)} + 1.0f;
    const f32x2 h = uu * f32x2{__builtin_amdgcn_rcpf(d.x), __builtin_amdgcn_rcpf(d.y)};
    hp[j >> 1] = cvtpk(h.x, h.y);
  }
  return v4u{hp[0], hp[1], hp[2], hp[3]};
}
__device__ __forceinline__ void g1_store_h(const Params& p, v4u hv, int panel, int chunk, unsigned w, int lane, int i) {
  const int tt = i >> 1, h2 = i & 1, t5 = lane & 31, hh = lane >> 5;
  const unsigned ttile = (unsigned)(panel * 16 + (w >> 1) * 4 + 2 * tt + (t5 >> 4)), s = (unsigned)(2 * chunk + (w & 1));
  __builtin_amdgcn_raw_buffer_store_b128(hv, mkrsrc(p.ws, WS_END), (int)(WS_H + (ttile * 56 + s) * 1024 + ((2 * h2 + hh) * 16 + (t5 & 15)) * 16), 0, AUX_SC1);
}

__device__ __forceinline__ void pf_issue(const Params& p, int lane, int m) {
  const int F = 8 * m + 4 * (lane >> 5);
  const char* srcb = (const char*)p.wd + (size_t)(32 * ((F % 224) >> 2) + (lane & 31)) * (DM * 2) + (F / 224) * 128;
  touch(srcb);
}
constexpr unsigned KG_ROW = 272;
constexpr unsigned KG_UNIT = 57 * KG_ROW;
constexpr unsigned KG_TB = 98304;
constexpr unsigned KG_KC = 155648;
constexpr unsigned KG_KCCH = 643 * 4 + 4;
constexpr unsigned KG_REV = 98304 + 33280;
constexpr unsigned KG_REVCH = 4160;
static_assert(KG_TB + 4 * KG_UNIT <= 163840 - 512 && KG_KC + 2 * KG_KCCH <= 163840 - 512, "LDS");

typedef f32x2 cf;
#define CM_MUL(t, a, b) "v_pk_mul_f32 " t ", " a ", " b " op_sel:[1,1] op_sel_hi:[1,0] neg_lo:[1,0]\n\t"
#define CM_FMA(r, a, b, t) "v_pk_fma_f32 " r ", " a ", " b ", " t " op_sel:[0,0,0] op_sel_hi:[0,1,1]\n\t"
__device__ __forceinline__ cf cmul(cf a, cf b) {
  cf r;
  asm(CM_MUL("%0", "%1", "%2") CM_FMA("%0", "%1", "%2", "%0") : "=&v"(r) : "v"(a), "v"(b));
  return r;
}
__device__ __forceinline__ void cmul2(cf& r0, cf a0, cf b0, cf& r1, cf a1, cf b1) {
  asm(CM_MUL("%0", "%2", "%3") CM_MUL("%1", "%4", "%5") CM_FMA("%0", "%2", "%3", "%0") CM_FMA("%1", "%4", "%5", "%1")
      : "=&v"(r0), "=&v"(r1) : "v"(a0), "v"(b0), "v"(a1), "v"(b1));
}
__device__ __forceinline__ void cmul4(cf& r0, cf& r1, cf& r2, cf& r3, cf a0, cf a1, cf a2, cf a3, cf b) {
  asm(CM_MUL("%0", "%4", "%8") CM_MUL("%1", "%5", "%8") CM_MUL("%2", "%6", "%8") CM_MUL("%3", "%7", "%8")
      CM_FMA("%0", "%4", "%8", "%0") CM_FMA("%1", "%5", "%8", "%1") CM_FMA("%2", "%6", "%8", "%2") CM_FMA("%3", "%7", "%8", "%3")
      : "=&v"(r0), "=&v"(r1), "=&v"(r2), "=&v"(r3) : "v"(a0), "v"(a1), "v"(a2), "v"(a3), "v"(b));
}
__device__ __forceinline__ cf cexp_e(float ar2, float ai2, float e) {
  const float m = __builtin_amdgcn_exp2f(ar2 * e);
  const float ph = __builtin_amdgcn_fractf(ai2 * e);
  return cf{m * __builtin_amdgcn_cosf(ph), m * __builtin_amdgcn_sinf(ph)};
}

struct KgenIn { unsigned are, aim, bre, bim, cre, cim, ldt; };
__device__ __forceinline__ void kgen_load(const Params& p, int ch0, unsigned w, int lane, KgenIn& in) {
  const int c = (w & 3) >> 1, mh = w & 1;
  const unsigned off = (unsigned)(((ch0 + c) * NM + 64 * mh + lane) * 2);
  const unsigned offd = (unsigned)((ch0 + c) * 2);
  asm volatile("global_load_ushort %0, %1, %2" : "=v"(in.are) : "v"(off), "s"(p.Are) : "memory");
  asm volatile("global_load_ushort %0, %1, %2" : "=v"(in.aim) : "v"(off), "s"(p.Aim) : "memory");
  asm volatile("global_load_ushort %0, %1, %2" : "=v"(in.ldt) : "v"(offd), "s"(p.logdt) : "memory");
  asm volatile("global_load_ushort %0, %1, %2" : "=v"(in.bre) : "v"(off), "s"(p.Bre) : "memory");
  asm volatile("global_load_ushort %0, %1, %2" : "=v"(in.bim) : "v"(off), "s"(p.Bim) : "memory");
  asm volatile("global_load_ushort %0, %1, %2" : "=v"(in.cre) : "v"(off), "s"(p.Cre) : "memory");
  asm volatile("global_load_ushort %0, %1, %2" : "=v"(in.cim) : "v"(off), "s"(p.Cim) : "memory");
}
__device__ __forceinline__ void epoch_load(const Params& p, unsigned& e) {
  asm volatile("global_load_dword %0, %1, %2 sc1" : "=v"(e) : "v"(WS_EPOCH), "s"(p.ws) : "memory");
}
__device__ __forceinline__ void kgen_load_plain(const Params& p, int ch0, unsigned w, int lane, KgenIn& in) {
  const int c = (w & 3) >> 1, idx = (ch0 + c) * NM + 64 * (w & 1) + lane;
  in.are = p.Are[idx]; in.aim = p.Aim[idx]; in.bre = p.Bre[idx]; in.bim = p.Bim[idx]; in.cre = p.Cre[idx]; in.cim = p.Cim[idx]; in.ldt = p.logdt[ch0 + c];
}
template <int N> __device__ __forceinline__ void kgen_wait(KgenIn& in) {
  asm volatile("s_waitcnt vmcnt(%7)" : "+v"(in.are), "+v"(in.aim), "+v"(in.bre), "+v"(in.bim), "+v"(in.cre), "+v"(in.cim), "+v"(in.ldt) : "i"(N) : "memory");
}

template <int R0, int R1, int R2, int R3>
__device__ __forceinline__ void kg_put4(unsigned tb, cf a, cf b, cf c, cf d) {
  unsigned t0, t1, t2, t3;
  asm volatile("v_cvt_pk_bf16_f32 %0, %5, %6\n\tv_cvt_pk_bf16_f32 %1, %7, %8\n\tv_cvt_pk_bf16_f32 %2, %9, %10\n\tv_cvt_pk_bf16_f32 %3, %11, %12\n\t"
               "ds_write_b32 %4, %0 offset:%13\n\tds_write_b32 %4, %1 offset:%14\n\tds_write_b32 %4, %2 offset:%15\n\tds_write_b32 %4, %3 offset:%16"
               : "=&v"(t0), "=&v"(t1), "=&v"(t2), "=&v"(t3)
               : "v"(tb), "v"(a.x), "v"(a.y), "v"(b.x), "v"(b.y), "v"(c.x), "v"(c.y), "v"(d.x), "v"(d.y),
                 "i"(R0 * KG_ROW), "i"(R1 * KG_ROW), "i"(R2 * KG_ROW), "i"(R3 * KG_ROW) : "memory");
}
template <int R0, int R1, int R2>
__device__ __forceinline__ void kg_put3(unsigned tb, cf a, cf b, cf c) {
  unsigned t0, t1, t2;
  asm volatile("v_cvt_pk_bf16_f32 %0, %4, %5\n\tv_cvt_pk_bf16_f32 %1, %6, %7\n\tv_cvt_pk_bf16_f32 %2, %8, %9\n\t"
               "ds_write_b32 %3, %0 offset:%10\n\tds_write_b32 %3, %1 offset:%11\n\tds_write_b32 %3, %2 offset:%12"
               : "=&v"(t0), "=&v"(t1), "=&v"(t2)
               : "v"(tb), "v"(a.x), "v"(a.y), "v"(b.x), "v"(b.y), "v"(c.x), "v"(c.y), "i"(R0 * KG_ROW), "i"(R1 * KG_ROW), "i"(R2 * KG_ROW) : "memory");
}
__device__ __forceinline__ void cmul3(cf& r0, cf& r1, cf& r2, cf a0, cf a1, cf a2, cf b) {
  asm(CM_MUL("%0", "%3", "%6") CM_MUL("%1", "%4", "%6") CM_MUL("%2", "%5", "%6") CM_FMA("%0", "%3", "%6", "%0") CM_FMA("%1", "%4", "%6", "%1") CM_FMA("%2", "%5", "%6", "%2")
      : "=&v"(r0), "=&v"(r1), "=&v"(r2) : "v"(a0), "v"(a1), "v"(a2), "v"(b));
}
__device__ __forceinline__ void kgen_unit(const KgenIn& in, unsigned w, int lane) {
  const unsigned ku = KG_TB + w * KG_UNIT;
  const unsigned tb = ku + lane * 4;
  const int j = lane & 15, g = lane >> 4;
  const bool lo = g < 2;
  const unsigned ba = ku + (41 + j) * KG_ROW + 16 * g;
  const unsigned aa0 = ku + j * KG_ROW + 16 * g, aa2 = ku + min(32 + j, 40) * KG_ROW + 16 * g;
  const unsigned o0 = ku + 4 * (128 * (g >> 1) + 4 * (g & 1) + 8 * j);
  const unsigned o1 = ku + 4 * (256 + 64 * g + (lo ? 4 : 2) * j), s1 = lo ? 8 : 128;
  const unsigned o2 = ku + 4 * (lo ? 512 + 64 * g + j : 642), d2 = lo ? 64 : 0;
  const unsigned f2 = (g == 2 && j == 0) ? ku + 4 * 640 : o2;
  const float dt = rbf(expf(bflo(in.ldt)));
  const float ar2 = (dt * bflo(in.are)) * 1.4426950408889634f;
  const float ai2 = (dt * bflo(in.aim)) * 0.15915494309189535f;
  const cf W = cmul(cf{bflo(in.cre), bflo(in.cim)}, cf{dt * bflo(in.bre), dt * bflo(in.bim)});
  const cf q1 = cexp_e(ar2, ai2, 128.f), z1 = cexp_e(ar2, ai2, 1.f), e1 = cexp_e(ar2, -ai2, 8.f);
  cf z2, e2, T1, q2;
  cmul2(z2, z1, z1, e2, e1, e1);
  cmul2(T1, W, q1, q2, q1, q1);
  cf z3, z4, e3, e4, q4, T2, T3;
  cmul2(z3, z2, z1, z4, z2, z2);
  cmul2(e3, e2, e1, e4, e2, e2);
  cmul3(T2, T3, q4, W, T1, q2, q2);
  kg_put4<0, 8, 41, 42>(tb, W, T1, cf{1.f, 0.f}, e1);
  cf z5, z6, z7, e5, e6, e7, e8, q8, T4, T5, T6, T7;
  cmul3(z5, z6, z7, z1, z2, z3, z4);
  cmul4(e5, e6, e7, e8, e1, e2, e3, e4, e4);
  cmul4(T4, T5, T6, T7, W, T1, T2, T3, q4);
  q8 = cmul(q4, q4);
  kg_put4<16, 20, 43, 44>(tb, T2, T3, e2, e3);
  cf a[4], b[4], a2[4], b2[4];
  cmul4(a[0], a[1], a[2], a[3], z1, z2, z3, z4, W);
  cmul4(b[0], b[1], b[2], b[3], z1, z2, z3, z4, T1);
  kg_put4<45, 46, 47, 48>(tb, e4, e5, e6, e7);
  kg_put4<24, 26, 28, 30>(tb, T4, T5, T6, T7);
  cmul4(a2[0], a2[1], a2[2], a2[3], z5, z6, z7, q8, W);
  cmul4(b2[0], b2[1], b2[2], b2[3], z5, z6, z7, q8, T1);
  kg_put4<1, 2, 3, 4>(tb, a[0], a[1], a[2], a[3]);
  kg_put4<9, 10, 11, 12>(tb, b[0], b[1], b[2], b[3]);
  const cf T8 = a2[3];
  cmul4(a[0], a[1], a[2], a[3], z2, z4, z6, q8, T2);
  cmul4(b[0], b[1], b[2], b[3], z2, z4, z6, q8, T3);
  kg_put4<5, 6, 7, 32>(tb, a2[0], a2[1], a2[2], a2[3]);
  kg_put4<13, 14, 15, 33>(tb, b2[0], b2[1], b2[2], b2[3]);
  cmul4(a2[0], a2[1], a2[2], a2[3], T4, T5, T6, T7, z4);
  cmul4(b2[0], b2[1], b2[2], b2[3], T4, T5, T6, T7, q8);
  kg_put4<17, 18, 19, 34>(tb, a[0], a[1], a[2], a[3]);
  kg_put4<21, 22, 23, 35>(tb, b[0], b[1], b[2], b[3]);
  cmul4(a[0], a[1], a[2], a[3], e1, e2, e3, e4, e8);
  cmul4(b[0], b[1], b[2], b[3], e5, e6, e7, T8, e8);
  const cf T16 = cmul(T8, q8);
  kg_put4<25, 27, 29, 31>(tb, a2[0], a2[1], a2[2], a2[3]);
  kg_put4<36, 37, 38, 39>(tb, b2[0], b2[1], b2[2], b2[3]);
  kg_put4<50, 51, 52, 53>(tb, a[0], a[1], a[2], a[3]);
  kg_put3<54, 55, 56>(tb, b[0], b[1], b[2]);
  {
    unsigned t0, t1;
    asm volatile("v_cvt_pk_bf16_f32 %0, %3, %4\n\tv_cvt_pk_bf16_f32 %1, %5, %6\n\tds_write_b32 %2, %0 offset:%7\n\tds_write_b32 %2, %1 offset:%8"
                 : "=&v"(t0), "=&v"(t1) : "v"(tb), "v"(e8.x), "v"(e8.y), "v"(T16.x), "v"(T16.y), "i"(49 * KG_ROW), "i"(40 * KG_ROW) : "memory");
  }
  v4u B[4], A[3][4];
#pragma unroll
  for (int s = 0; s < 4; ++s) { B[s] = ldsrd128(ba + 64 * s); A[0][s] = ldsrd128(aa0 + 64 * s); A[1][s] = ldsrd128(aa0 + 16 * KG_ROW + 64 * s); A[2][s] = ldsrd128(aa2 + 64 * s); }
  SBAR();
  f32x4 acc[3];
#pragma unroll
  for (int t = 0; t < 3; ++t) acc[t] = f32x4{0.f, 0.f, 0.f, 0.f};
#pragma unroll
  for (int s = 0; s < 4; ++s)
#pragma unroll
    for (int t = 0; t < 3; ++t) acc[t] = mfma16(A[t][s], B[s], acc[t]);
#pragma unroll
  for (int v = 0; v < 4; ++v) ldswrf(o0 + 4 * v, acc[0][v]);
  ldswrf(o1, acc[1][0]); ldswrf(o1 + 4, acc[1][1]); ldswrf(o1 + s1, acc[1][2]); ldswrf(o1 + s1 + 4, acc[1][3]);
  ldswrf(f2, acc[2][0]); ldswrf(o2 + d2, acc[2][1]); ldswrf(o2 + 2 * d2, acc[2][2]); ldswrf(o2 + 3 * d2, acc[2][3]);
  if (lane == 0) ldswrf(ku + 641 * 4, 0.f);
}
__device__ __forceinline__ void kgen_sum() {
  const int tid = threadIdx.x, c = tid >> 8, i = tid & 255;
  const unsigned src = KG_TB + 2 * c * KG_UNIT + 4 * i, dst = KG_KC + c * KG_KCCH + 4 * i;
  const float a0 = ldsrdf(src) + ldsrdf(src + KG_UNIT), a1 = ldsrdf(src + 1024) + ldsrdf(src + 1024 + KG_UNIT);
  ldswrf(dst, a0); ldswrf(dst + 1024, a1);
  if (i < 130) ldswrf(dst + 2048, ldsrdf(src + 2048) + ldsrdf(src + 2048 + KG_UNIT));
}
__device__ __forceinline__ void kgen_expand(v4u li) {
  const int cI = threadIdx.x & 255;
#pragma unroll
  for (int c = 0; c < 2; ++c) {
    const unsigned kc = KG_KC + c * KG_KCCH;
    float v[8];
#pragma unroll
    for (int i = 0; i < 4; ++i) { v[7 - 2 * i] = ldsrdf(kc + (li[i] & 0xffff)); v[6 - 2 * i] = ldsrdf(kc + (li[i] >> 16)); }
    ldswr128(KG_REV + c * KG_REVCH + 16 * (cI + 1), v4u{cvtpk(v[0], v[1]), cvtpk(v[2], v[3]), cvtpk(v[4], v[5]), cvtpk(v[6], v[7])});
    if (cI == 0) ldswr128(KG_REV + c * KG_REVCH, v4u{0, 0, 0, 0});
  }
}

constexpr unsigned CV_COPY = 4160;
__device__ __forceinline__ constexpr unsigned cv_k(int c) { return c ? 98304u : 0u; }
constexpr unsigned CV_U = 33280;
constexpr unsigned CV_UCH = 4608;
constexpr unsigned CV_RED = 98304 + 33280;

struct LidxTable {
  u16 v[2056];
  constexpr LidxTable() : v() {
    for (int i = 0; i < 2056; ++i) {
      const int l = i - 8;
      int idx = 641;
      if (l >= 0) {
        int s = 0;
        while ((l >> s) >= 256) ++s;
        int m = l;
        if (s > 0) m = (l + (1 << (s - 1)) - 1 + ((l >> s) & 1)) >> s;
        idx = m + 128 * s;
      }
      v[i] = (u16)(idx * 4);
    }
  }
};
__device__ const LidxTable g_lidx = LidxTable();

__device__ __forceinline__ v4u kgen_load_idx() {
  return ((const GAS v4u*)&g_lidx)[(threadIdx.x & 255) + 1];
}
__device__ __forceinline__ void conv_copies() {
  const int tid = threadIdx.x, c = tid >> 8, cI = tid & 255;
  const v4u own = ldsrd128(KG_REV + c * KG_REVCH + 16 * (cI + 1)), nb = ldsrd128(KG_REV + c * KG_REVCH + 16 * cI);
  const unsigned d[8] = {own.x, own.y, own.z, own.w, nb.x, nb.y, nb.z, nb.w};
  unsigned e[7];
#pragma unroll
  for (int k = 0; k < 7; ++k) e[k] = __builtin_amdgcn_alignbyte(d[k + 1], d[k], 2);
  const unsigned o = cv_k(c) + 16 * (cI + 1);
#pragma unroll
  for (int k = 0; k < 4; ++k) {
    ldswr128(o + (7 - 2 * k) * CV_COPY, v4u{d[k], d[k + 1], d[k + 2], d[k + 3]});
    ldswr128(o + (6 - 2 * k) * CV_COPY, v4u{e[k], e[k + 1], e[k + 2], e[k + 3]});
  }
  if (tid < 16) ldswr128(cv_k(tid >> 3) + (tid & 7) * CV_COPY, v4u{0, 0, 0, 0});
}
struct ConvIn { v4u u; float dsk; };
__device__ __forceinline__ void conv_load(const Params& p, int ch0, unsigned w, ConvIn& in) {
  const int tid = threadIdx.x, cc = tid & 255;
  const rsrc_t rsS = mkrsrc(p.ws, WS_END);
  in.u = bld128(rsS, (unsigned)(WS_UT + (cc >> 3) * TB_PITCH + (ch0 + (tid >> 8)) * 128 + (cc & 7) * 16), 0, 1);
  in.dsk = bflo(p.Dskip[ch0 + (w >> 2)]);
}
__device__ __forceinline__ void conv_fill(const ConvIn& in) {
  const int tid = threadIdx.x, c = tid >> 8, cc = tid & 255;
  ldswr128(CV_U + c * CV_UCH + 512 + cc * 16, in.u);
  if (tid >= 64 && tid < 128) ldswr128(CV_U + ((tid - 64) >> 5) * CV_UCH + (tid & 31) * 16, v4u{0, 0, 0, 0});
}

template <int WQ> struct ConvSet {
  static constexpr int NA = WQ == 0 ? 8 : 7;
  static constexpr int q(int k) { return WQ == 0 ? (k == 0 ? 7 : k == 1 ? 4 : k == 2 ? 3 : 0) : (k == 0 ? 6 : k == 1 ? 5 : k == 2 ? 2 : 1); }
};
template <int WQ, int R, int A, class H>
__device__ __forceinline__ void conv_step(unsigned abase, unsigned bbase, v4u (&fa)[8], v4u (&fb)[8], f32x4 (&acc)[4], H& hook) {
  using S = ConvSet<WQ>;
  hook.template at<R * S::NA + A>();
#pragma unroll
  for (int k = 0; k < 4; ++k)
    if (S::q(k) >= A) acc[k] = mfma16(fa[A], fb[S::q(k) - A], acc[k]);
  if constexpr (R + 1 < 4) {
    fb[S::NA - 1 - A] = ldsrd128(bbase + 512 * (S::NA - 1 - A) + 64 * (7 - (R + 1)));
    fa[A] = ldsrd128(abase + 512 * A + 64 * (R + 1));
  }
  SBAR();
  if constexpr (A + 1 < S::NA) conv_step<WQ, R, A + 1>(abase, bbase, fa, fb, acc, hook);
  else if constexpr (R + 1 < 4) conv_step<WQ, R + 1, 0>(abase, bbase, fa, fb, acc, hook);
}
template <int WQ, class H>
__device__ __forceinline__ void conv_mfma(unsigned abase, unsigned bbase, f32x4 (&acc)[4], H& hook) {
  using S = ConvSet<WQ>;
  v4u fa[8], fb[8];
#pragma unroll
  for (int a = 0; a < S::NA; ++a) { fb[S::NA - 1 - a] = ldsrd128(bbase + 512 * (S::NA - 1 - a) + 64 * 7); fa[a] = ldsrd128(abase + 512 * a); }
  SBAR();
  conv_step<WQ, 0, 0>(abase, bbase, fa, fb, acc, hook);
}

struct ConvAcc { f32x4 keep[2]; };
template <class H>
__device__ __forceinline__ void conv_main(unsigned w, int lane, ConvAcc& ca, H& hook) {
  const int c = w >> 2, wq = w & 1, wr = (w >> 1) & 1;
  const int r = lane & 15, h = lane >> 4;
  const unsigned abase = cv_k(c) + (r & 7) * CV_COPY + 16 * ((r >> 3) + h) + 256 * wr;
  const unsigned bbase = CV_U + c * CV_UCH + 16 * (2 * r - h + 33) - 448 - 256 * wr;
  f32x4 acc[4];
#pragma unroll
  for (int k = 0; k < 4; ++k) acc[k] = f32x4{0.f, 0.f, 0.f, 0.f};
  if (wq == 0) conv_mfma<0>(abase, bbase, acc, hook); else conv_mfma<1>(abase, bbase, acc, hook);
  const unsigned mine = CV_RED + w * 2048 + lane * 16;
  const f32x4 a0 = acc[0], a1 = acc[1], a2 = acc[2], a3 = acc[3];
  *(LAS f32x4*)(uintptr_t)(mine) = wr == 0 ? a2 : a0;
  *(LAS f32x4*)(uintptr_t)(mine + 1024) = wr == 0 ? a3 : a1;
  ca.keep[0] = wr == 0 ? a0 : a2;
  ca.keep[1] = wr == 0 ? a1 : a3;
}
__device__ __forceinline__ void conv_finish(const Params& p, int ch0, unsigned w, int lane, const ConvAcc& ca, float dsk, unsigned tag) {
  const int c = w >> 2, wq = w & 1, wr = (w >> 1) & 1;
  const int r = lane & 15, h = lane >> 4;
  const unsigned other = CV_RED + (w ^ 2) * 2048 + lane * 16;
  const rsrc_t rsS = mkrsrc(p.ws, WS_END);
#pragma unroll
  for (int k = 0; k < 2; ++k) {
    const f32x4 o = *(const LAS f32x4*)(uintptr_t)(other + 1024 * k);
    const f32x4 m = ca.keep[k];
    const int kk = 2 * wr + k;
    const int q = wq == 0 ? (kk == 0 ? 7 : kk == 1 ? 4 : kk == 2 ? 3 : 0) : (kk == 0 ? 6 : kk == 1 ? 5 : kk == 2 ? 2 : 1);
    const int t = 256 * q + 16 * r + 4 * h;
    const v2u uu = ldsrd64(CV_U + c * CV_UCH + 512 + 2 * t);
    const unsigned y01 = cvtpk(m[0] + o[0], m[1] + o[1]), y23 = cvtpk(m[2] + o[2], m[3] + o[3]);
    const unsigned s01 = cvtpk(bflo(uu.x) * dsk, bfhi(uu.x) * dsk), s23 = cvtpk(bflo(uu.y) * dsk, bfhi(uu.y) * dsk);
    const v2u av = {cvtpk(bflo(y01) + bflo(s01), bfhi(y01) + bfhi(s01)), cvtpk(bflo(y23) + bflo(s23), bfhi(y23) + bfhi(s23))};
    __builtin_amdgcn_raw_buffer_store_b128(v4u{tag, av.x, av.y, tag ^ A_TAG2}, rsS, (int)(WS_AT + (t >> 6) * AT_PITCH + (ch0 + c) * 256 + (t & 63) * 4), 0, AUX_SC1);
  }
}
constexpr unsigned G2_RED = 0;
constexpr unsigned G2_AT = 8 * 8 * 2048;
constexpr unsigned G2_AROW = 144;

struct G2W {
  v4u wf[7][4];
  rsrc_t rs;
  unsigned voff, sw, sh;
  template <int I> __device__ __forceinline__ void load() { wf[I >> 2][I & 3] = bld128(rs, voff, sw + I * 1024, 1); }
};
__device__ __forceinline__ void g2_setup_w(const Params& p, int t0, int ct, unsigned w, int lane, G2W& g) {
  g.rs = mkrsrc(p.ws, WS_END);
  g.voff = (unsigned)(lane * 16);
  g.sw = (unsigned)(WS_WD + (ct * 56 + 7 * w) * 4096);
  g.sh = (unsigned)(WS_H + ((t0 / 16) * 56 + 7 * w) * 1024);
}
constexpr unsigned WT_LDS = 147456;
__device__ __forceinline__ void wdtile_dma(const Params& p, int F, unsigned w, int lane) {
  const int ct = F / 224, s = (F % 224) >> 2;
  glds(WT_LDS + w * 1024, (unsigned)((32 * s + 8 * (w & 3) + (lane >> 3)) * 1024 + ct * 128 + (lane & 7) * 16), mkrsrc(p.wd, FF * DM * 2), 0);
}
__device__ __forceinline__ void wdtile_store(const Params& p, int F, unsigned w, int lane) {
  const int dt = F & 3, g = lane >> 4, q = (lane & 15) >> 2, pp = lane & 3;
  const unsigned a = WT_LDS + (w >> 2) * 4096 + (16 * (g >> 1) + 4 * (g & 1) + q) * 128 + (32 * (dt >> 1) + 8 * pp + 4 * (dt & 1)) * 2;
  const v2u lo = trread(a), hi = trread(a + 8 * 128);
  __builtin_amdgcn_raw_buffer_store_b128(v4u{lo.x, lo.y, hi.x, hi.y}, mkrsrc(p.ws, WS_END), (int)(WS_WD + F * 1024 + lane * 16), 0, AUX_SC1);
}

struct G2S { f32x4 acc[4][4]; v4u hf[7][4]; };
template <int LO, int HI> __device__ __forceinline__ void g2_load_h(const G2W& g, G2S& st) {
  if constexpr (LO < HI) {
#pragma unroll
    for (int tt = 0; tt < 4; ++tt) st.hf[LO][tt] = bld128(g.rs, g.voff, g.sh + LO * 1024 + tt * (56 * 1024), 1);
    g2_load_h<LO + 1, HI>(g, st);
  } else {
    SBAR();
  }
}
template <int S> __device__ __forceinline__ void g2_step(G2S& st, const G2W& g) {
#pragma unroll
  for (int tt = 0; tt < 4; ++tt)
#pragma unroll
    for (int dt = 0; dt < 4; ++dt) st.acc[dt][tt] = mfma16(g.wf[S][dt], st.hf[S][tt], S == 0 ? f32x4{0.f, 0.f, 0.f, 0.f} : st.acc[dt][tt]);
  SBAR();
}
__device__ __forceinline__ void g2_last_step_scatter(G2S& st, const G2W& g, unsigned w, int lane) {
  const unsigned base = G2_RED + w * 2048 + lane * 16;
#pragma unroll
  for (int tt = 0; tt < 4; ++tt) {
#pragma unroll
    for (int dt = 0; dt < 4; ++dt) st.acc[dt][tt] = mfma16(g.wf[6][dt], st.hf[6][tt], st.acc[dt][tt]);
    if (tt > 0) {
#pragma unroll
      for (int dt = 0; dt < 4; ++dt) *(LAS f32x4*)(uintptr_t)(base + (2 * (tt - 1) + (dt >> 1)) * 16384 + (dt & 1) * 1024) = st.acc[dt][tt - 1];
    }
    SBAR();
  }
#pragma unroll
  for (int dt = 0; dt < 4; ++dt) *(LAS f32x4*)(uintptr_t)(base + (6 + (dt >> 1)) * 16384 + (dt & 1) * 1024) = st.acc[dt][3];
}
__device__ __forceinline__ void g2_finish(const Params& p, int t0, int ct, unsigned w, int lane, float gt) {
  const unsigned base = G2_RED + w * 16384 + lane * 16;
  f32x4 b0 = *(const LAS f32x4*)(uintptr_t)(base), b1 = *(const LAS f32x4*)(uintptr_t)(base + 1024);
#pragma unroll
  for (int s = 1; s < 8; ++s) {
    b0 += *(const LAS f32x4*)(uintptr_t)(base + s * 2048);
    b1 += *(const LAS f32x4*)(uintptr_t)(base + s * 2048 + 1024);
  }
  const int n2 = w & 1, tto = w >> 1;
  const int h = lane >> 4, q = (lane & 15) >> 2, pp = lane & 3;
  const unsigned aa = G2_AT + (32 * n2 + 8 * h + q) * G2_AROW + (16 * tto + 4 * pp) * 2;
  const v2u a0 = trread(aa), a1 = trread(aa + 4 * G2_AROW);
  const float g1 = rbf(1.0f - gt);
  const f32x4 c0 = b0 * g1, c1 = b1 * g1;
  const v4u ov = {cvtpk(__builtin_fmaf(gt, bflo(a0.x), c0[0]), __builtin_fmaf(gt, bfhi(a0.x), c0[1])), cvtpk(__builtin_fmaf(gt, bflo(a0.y), c0[2]), __builtin_fmaf(gt, bfhi(a0.y), c0[3])),
                  cvtpk(__builtin_fmaf(gt, bflo(a1.x), c1[0]), __builtin_fmaf(gt, bfhi(a1.x), c1[1])), cvtpk(__builtin_fmaf(gt, bflo(a1.y), c1[2]), __builtin_fmaf(gt, bfhi(a1.y), c1[3]))};
  *(GAS v4u*)((char*)p.out + (size_t)(t0 + 16 * tto + (lane & 15)) * (DM * 2) + (64 * ct + 32 * n2 + 8 * h) * 2) = ov;
}

__device__ __forceinline__ unsigned flag_ld(const Params& p, unsigned off) {
  return __hip_atomic_load((const GAS unsigned*)(p.ws + off), __ATOMIC_RELAXED, __HIP_MEMORY_SCOPE_AGENT);
}
__device__ __forceinline__ void flag_spin(const Params& p, unsigned v, unsigned off, unsigned tag) {
  if (!__all(v == tag)) {
    for (int it = 0; it < SPIN_CAP; ++it) {
      __builtin_amdgcn_s_sleep(4);
      if (__all(flag_ld(p, off) == tag)) break;
    }
  }
  __builtin_amdgcn_fence(__ATOMIC_ACQUIRE, "wavefront");
}
__device__ __forceinline__ unsigned w_flag_off(int ct, int lane) { return WS_FLAGW + (28 * ct + min(lane, 27)) * FLAG_STRIDE; }
__device__ __forceinline__ unsigned p_flag_off(int lane) { return WS_FLAGP + (lane & 31) * FLAG_STRIDE; }
struct G1Hook {
  const Params& p; G1S& st; G2W& gw; ConvIn& ci; v4u& li; unsigned tag; int lane; int ch0; unsigned w; int ct; int m; unsigned fw, fp;
  static constexpr int younger(int s) { return s == 1 ? 1 : s == 5 ? 2 : s == 6 ? 9 : 0; }
  template <int J> __device__ __forceinline__ void at() {
    if constexpr (J == 0) { wdtile_dma(p, 8 * m + w, w, lane); g1s_dma<2, 0>(st); g1s_dma<2, 4>(st); }
    if constexpr (J == 1) { g1s_dma<2, 1>(st); g1s_dma<2, 5>(st); }
    if constexpr (J == 2) { g1s_dma<2, 2>(st); g1s_dma<2, 3>(st); }
    if constexpr (J == 3) wdtile_store(p, 8 * m + w, w, lane);
    if constexpr (J == 11) { if (threadIdx.x == 0) flag_set(p.ws, WS_FLAGW + m * FLAG_STRIDE, tag); }
    if constexpr (J == 19) li = kgen_load_idx();
    if constexpr (J == 21) fw = flag_ld(p, w_flag_off(ct, lane));
    if constexpr (J == 23) flag_spin(p, fw, w_flag_off(ct, lane), tag);
    if constexpr (J >= 23 && J < 29) { gw.template load<2 * (J - 23)>(); gw.template load<2 * (J - 23) + 1>(); }
    if constexpr (J == 24) fp = flag_ld(p, p_flag_off(lane));
    if constexpr (J == 27) { flag_spin(p, fp, p_flag_off(lane), tag); conv_load(p, ch0, w, ci); }
    if constexpr (J == 30) conv_fill(ci);
  }
};
struct CvHook {
  const Params& p; G2W& gw; unsigned off; unsigned v;
  template <int I> __device__ __forceinline__ void at() {
    if constexpr (I < 16) gw.template load<12 + I>();
    if constexpr (I == 26) v = flag_ld(p, off);
  }
};
__device__ __forceinline__ unsigned h_flag_off(int xcd, int tg, unsigned w, int lane) {
  const int sj = 7 * (int)w + min(lane, 6), cj = sj >> 1;
  return WS_FLAGH + (8 * (4 * (cj % 7) + (xcd & 3)) + 2 * (cj / 7) + (xcd >> 2)) * FLAG_STRIDE + (2 * tg + (sj & 1)) * 16;
}

__global__ void __launch_bounds__(512, 2) k_fused(Params p) {
  const int b = blockIdx.x, xcd = b & 7, slot = b >> 3;
  const int tid = threadIdx.x, lane = tid & 63;
  const unsigned w = rfl(tid >> 6);
  if (w == 7) {
    unsigned long long pc;
    asm volatile("s_getpc_b64 %0" : "=s"(pc));
    const char* a = (const char*)(pc & ~127ull) + min(slot * 7 + min(lane, 6), 217) * 128;
    touch(a);
  }
  const int ch0 = 2 * b;
  const bool spare = slot >= 28;
  const int panel = 4 * (xcd & 1) + (slot & 3), chunk = 7 * (xcd >> 1) + (slot >> 2);
  const int ct = slot & 7, t0 = 256 * xcd + 64 * (slot >> 3);
  unsigned tag;
  G2W gw;
  g2_setup_w(p, t0, ct, w, lane, gw);
  ConvIn ci;
  v4u li;
  if (w == 7) {
    const char* a = lane < 24 ? (const char*)p.ws + min((unsigned)lane << 20, WS_END - 4) : lane < 26 ? (const char*)p.out + ((lane - 24) << 20) : (const char*)p.wd + min(lane - 26, 1) * (1 << 20);
    touch(a);
  }
  if (__builtin_expect(!spare, 1)) {
    G1S st;
    g1_setup(p, st, panel, chunk, w, lane);
    const int m = xcd * 28 + slot;
    unsigned ep;
    if (w < 4) {
      KgenIn kin;
      kgen_load(p, ch0, w, lane, kin);
      epoch_load(p, ep);
      asm volatile("s_waitcnt vmcnt(0)" : "+v"(ep) :: "memory");
      kgen_wait<0>(kin);
      kgen_unit(kin, w, lane);
    } else {
      epoch_load(p, ep);
      const unsigned wp = 2 * (w - 4);
      g1s_dma_stage_for<0>(st, wp); g1s_dma_stage_for<0>(st, wp + 1);
      pf_issue(p, lane, m);
      g1s_dma_stage_for<1>(st, wp); g1s_dma_stage_for<1>(st, wp + 1);
      asm volatile("s_waitcnt vmcnt(25)" : "+v"(ep) :: "memory");
    }
    tag = rfl(ep) + 1u;
    LGKM_BAR();
    kgen_sum();
    G1Hook gh{p, st, gw, ci, li, tag, lane, ch0, w, ct, m, 0u, 0u};
    g1_main(st, w, lane, gh);
    g1_store_h(p, g1_glu_tile(st, 0), panel, chunk, w, lane, 0);
    SBAR();
    g1_store_h(p, g1_glu_tile(st, 1), panel, chunk, w, lane, 1);
    SBAR();
    g1_store_h(p, g1_glu_tile(st, 2), panel, chunk, w, lane, 2);
    SBAR();
    g1_store_h(p, g1_glu_tile(st, 3), panel, chunk, w, lane, 3);
    SBAR();
  } else {
    const int j = xcd * 4 + (slot - 28);
    const unsigned ep = __hip_atomic_load((const GAS unsigned*)(p.ws + WS_EPOCH), __ATOMIC_RELAXED, __HIP_MEMORY_SCOPE_AGENT);
    pre_phase(p, j);
    tag = rfl(ep) + 1u;
    WAIT_VM_LGKM_BAR(0);
    if (tid == 0) flag_set(p.ws, WS_FLAGP + j * FLAG_STRIDE, tag);
    li = kgen_load_idx();
    if (w < 4) { KgenIn kin; kgen_load_plain(p, ch0, w, lane, kin); kgen_unit(kin, w, lane); }
    LGKM_BAR();
    kgen_sum();
    LGKM_BAR();
    flag_spin(p, flag_ld(p, p_flag_off(lane)), p_flag_off(lane), tag);
    conv_load(p, ch0, w, ci);
    conv_fill(ci);
    flag_spin(p, flag_ld(p, w_flag_off(ct, lane)), w_flag_off(ct, lane), tag);
    if (b == 224 && w == 7) {
      for (int it = 0; it < SPIN_CAP; ++it) {
        bool ok = true;
#pragma unroll
        for (int k = 0; k < 4; ++k) ok &= flag_ld(p, WS_FLAGW + min(64 * k + lane, 223) * FLAG_STRIDE) == tag;
        if (__all(ok)) break;
        __builtin_amdgcn_s_sleep(1);
      }
      if (lane == 0) flag_set(p.ws, WS_EPOCH, tag);
    }
    gw.load<0>(); gw.load<1>(); gw.load<2>(); gw.load<3>(); gw.load<4>(); gw.load<5>(); gw.load<6>(); gw.load<7>(); gw.load<8>(); gw.load<9>();
    gw.load<10>(); gw.load<11>();
    __builtin_amdgcn_s_waitcnt(0x0F70);
  }
  if (w < 4) kgen_expand(li);
  LGKM_BAR();
  conv_copies();
  WAIT_VM(0);
  if (!spare && lane == 0) flag_set(p.ws, WS_FLAGH + b * FLAG_STRIDE + w * 16, tag);
  LGKM_BAR();
  const unsigned hoff = h_flag_off(xcd, (t0 >> 6) & 3, w, lane);
  CvHook ch{p, gw, hoff, 0u};
  ConvAcc ca;
  conv_main(w, lane, ca, ch);
  LGKM_BAR();
  conv_finish(p, ch0, w, lane, ca, ci.dsk, tag);
  SBAR();
  flag_spin(p, ch.v, hoff, tag);
  G2S st2;
  g2_load_h<0, 3>(gw, st2);
  g2_step<0>(st2, gw);
  g2_load_h<3, 5>(gw, st2);
  g2_step<1>(st2, gw);
  g2_load_h<5, 7>(gw, st2);
  g2_step<2>(st2, gw);
  g2_step<3>(st2, gw);
  g2_step<4>(st2, gw);
  const rsrc_t rsS = mkrsrc(p.ws, WS_END);
  const unsigned aoff = (unsigned)(WS_AT + (t0 >> 6) * AT_PITCH + (64 * ct + (tid >> 3)) * 256 + (tid & 7) * 32);
  v4u ag0 = bld128(rsS, aoff, 0, 1), ag1 = bld128(rsS, aoff + 16, 0, 1);
  const float gt = __uint_as_float(__builtin_amdgcn_raw_buffer_load_b32(rsS, (unsigned)(WS_G + (t0 + 16 * (w >> 1) + (lane & 15)) * 4), 0, AUX_SC1));
  SBAR();
  g2_step<5>(st2, gw);
  LGKM_BAR();
  g2_last_step_scatter(st2, gw, w, lane);
  if (!__all(ag0.x == tag && ag0.w == (tag ^ A_TAG2) && ag1.x == tag && ag1.w == (tag ^ A_TAG2))) {
    for (int it = 0; it < SPIN_CAP; ++it) {
      __builtin_amdgcn_s_sleep(4);
      ag0 = bld128(rsS, aoff, 0, 1); ag1 = bld128(rsS, aoff + 16, 0, 1);
      if (__all(ag0.x == tag && ag0.w == (tag ^ A_TAG2) && ag1.x == tag && ag1.w == (tag ^ A_TAG2))) break;
    }
  }
  ldswr128(G2_AT + (tid >> 3) * G2_AROW + (tid & 7) * 16, v4u{ag0.y, ag0.z, ag1.y, ag1.z});
  LGKM_BAR();
  g2_finish(p, t0, ct, w, lane, gt);
}

extern "C" void kernel_launch(void* const* d_in, const int* in_sizes, int n_in, void* d_out, int out_size, void* d_ws, size_t ws_size, hipStream_t stream) {
  Params p;
  p.x = (const u16*)d_in[0]; p.Are = (const u16*)d_in[1]; p.Aim = (const u16*)d_in[2]; p.Bre = (const u16*)d_in[3]; p.Bim = (const u16*)d_in[4];
  p.Cre = (const u16*)d_in[5]; p.Cim = (const u16*)d_in[6]; p.logdt = (const u16*)d_in[7]; p.Dskip = (const u16*)d_in[8];
  p.wg = (const u16*)d_in[10]; p.wu = (const u16*)d_in[11]; p.wd = (const u16*)d_in[12]; p.gw = (const u16*)d_in[13];
  p.out = (u16*)d_out; p.ws = (unsigned char*)d_ws;
  (void)hipFuncSetAttribute((const void*)k_fused, hipFuncAttributeMaxDynamicSharedMemorySize, 160 * 1024);
  k_fused<<<256, 512, 160 * 1024, stream>>>(p);
}
```
